```python
import torch

def _softmax(x, axis=-1):
    return torch.softmax(x, dim=axis)

def _gelu(x):
    return torch.nn.functional.gelu(x, approximate='tanh')

def _silu(x):
    return torch.nn.functional.silu(x)

def _sigmoid(x):
    return torch.sigmoid(x)

def _layernorm(x, axis=-1, eps=1e-05):
    m = x.mean(dim=axis, keepdim=True)
    v = x.var(dim=axis, keepdim=True, unbiased=False)
    return (x - m) / torch.sqrt(v + eps)

def _scatter_add(tgt, idx, src, axis=0):
    return tgt.clone().index_add_(axis, idx, src)

def _segment_sum(src, seg, n):
    out = torch.zeros((n,) + src.shape[1:], dtype=src.dtype)
    return out.index_add_(0, seg, src)

def _pad(x, pw):
    flat = tuple((v for pair in reversed(pw) for v in pair))
    return torch.nn.functional.pad(x, flat)
kernel_name = 'gated_mix__ssm_conv_s4d__mixer_gmlp__prompt'

def setup_inputs():

    _w = lambda a: (lambda t: t.to(torch.bfloat16) if t.is_floating_point() else t)(torch.as_tensor(a))

    def _A_setup():
        Bsz, L, D, N = (1, 2048, 576, 128)
        return {'u': _w((lambda *s: torch.randn(*s, dtype=torch.bfloat16) * 0.05)(Bsz, L, D)), 'log_A_real': _w((lambda *s: torch.randn(*s, dtype=torch.bfloat16) * 3.2)(D, N) * 0.5), 'A_imag': _w((lambda *s: torch.randn(*s, dtype=torch.bfloat16) * 0.05)(D, N) * 3.14), 'C_real': _w((lambda *s: torch.randn(*s, dtype=torch.bfloat16) * 3.2)(D, N) / N ** 0.5), 'C_imag': _w((lambda *s: torch.randn(*s, dtype=torch.bfloat16) * 3.2)(D, N) / N ** 0.5), 'log_dt': _w((lambda *s: torch.randn(*s, dtype=torch.bfloat16) * 0.05)(D) * 0.5 - 4.0), 'D_skip': _w((lambda *s: torch.randn(*s, dtype=torch.bfloat16) * 0.4)(D))}
    _A_in = _A_setup()

    def _B_setup():
        B, S, D, F = (1, 2048, 576, 4608)
        Fh = F // 2
        return {'x': _w((lambda *s: torch.randn(*s, dtype=torch.bfloat16) * 0.05)(B, S, D)), 'w_in': _w((lambda *s: torch.randn(*s, dtype=torch.bfloat16) * 0.2)(D, 2 * Fh)), 'w_sp': _w(0.01 * (lambda *s: torch.randn(*s, dtype=torch.bfloat16) * 0.2)(S, S)), 'b_sp': _w(1.0 + 0.01 * (lambda *s: torch.randn(*s, dtype=torch.bfloat16) * 0.05)(S)), 'w_out': _w((lambda *s: torch.randn(*s, dtype=torch.bfloat16) * 0.05)(Fh, D))}
    _B_in = _B_setup()
    return {'A_u': _A_in['u'], 'A_log_A_real': _A_in['log_A_real'], 'A_A_imag': _A_in['A_imag'], 'A_C_real': _A_in['C_real'], 'A_C_imag': _A_in['C_imag'], 'A_log_dt': _A_in['log_dt'], 'A_D_skip': _A_in['D_skip'], 'B_x': _B_in['x'], 'B_w_in': _B_in['w_in'], 'B_w_sp': _B_in['w_sp'], 'B_b_sp': _B_in['b_sp'], 'B_w_out': _B_in['w_out'], 'gate_w': _w((lambda *s: torch.randn(*s, dtype=torch.bfloat16) * 0.05)(576) * 16.0)}

def reference(A_u, A_log_A_real, A_A_imag, A_C_real, A_C_imag, A_log_dt, A_D_skip, B_x, B_w_in, B_w_sp, B_b_sp, B_w_out, gate_w):

    def _A_ref(u, log_A_real, A_imag, C_real, C_imag, log_dt, D_skip):
        L = u.shape[1]
        dt = torch.exp(log_dt)
        A = -torch.exp(log_A_real) + 1j * A_imag
        C = (C_real + 1j * C_imag) * dt[:, None]
        pos = (lambda x, dt: x.to(dt))(torch.arange(L), log_dt.dtype)
        P = torch.exp((A * dt[:, None])[:, :, None] * pos[None, None, :])
        K = 2.0 * torch.einsum('dn,dnl->dl', C, P).real
        L2 = 2 * L
        u_t = (lambda x, *d: x.permute(*d))(u, 0, 2, 1)
        Kf = (lambda x, *a, **k: torch.fft.rfft(x.float(), *a, **k))(K, L2)
        Uf = (lambda x, *a, **k: torch.fft.rfft(x.float(), *a, **k))(u_t, L2)
        y = (lambda x, *a, **k: torch.fft.irfft(x, *a, **k).to(torch.bfloat16))(Uf * Kf[None, :, :], L2)[:, :, :L]
        y = (lambda x, *d: x.permute(*d))(y, 0, 2, 1)
        return y + u * D_skip[None, None, :]

    def _B_ref(x, w_in, w_sp, b_sp, w_out):
        B, S, D = x.shape
        F = w_in.shape[-1]
        Fh = F // 2
        z = _gelu(torch.einsum('bsd,df->bsf', x, w_in))
        z1 = z[..., :Fh].clone()
        z2 = _layernorm(z[..., Fh:], axis=-1)
        g = torch.einsum('ts,bsf->btf', w_sp, z2) + b_sp[None, :, None]
        return torch.einsum('bsf,fd->bsd', z1 * g, w_out)
    x = A_u
    g = _sigmoid((x * gate_w).sum(-1, keepdims=True))
    a = _A_ref(x, A_log_A_real, A_A_imag, A_C_real, A_C_imag, A_log_dt, A_D_skip)
    b = _B_ref(x, B_w_in, B_w_sp, B_b_sp, B_w_out)
    return g * a + (1 - g) * b
```

```cpp
#include <hip/hip_runtime.h>
#include <hip/hip_bf16.h>
typedef unsigned short u16;
__device__ inline float bf2f(u16 v){ return __uint_as_float(((unsigned)v)<<16); }
__device__ inline u16 f2bf(float f){ unsigned u=__float_as_uint(f); u += 0x7FFFu + ((u>>16)&1u); return (u16)(u>>16); }
__device__ inline float rbf(float f){ return bf2f(f2bf(f)); }
constexpr int L=2048, D=576, N=128, F=4608, FH=2304;

__global__ void k_gate(const u16* x, const u16* gw, float* gate){
  int s = blockIdx.x; int lane = threadIdx.x; float a=0;
  for(int d=lane; d<D; d+=64) a += rbf(bf2f(x[s*D+d])*bf2f(gw[d]));
  for(int o=32;o>=1;o>>=1) a += __shfl_xor(a,o);
  if(lane==0){ a = rbf(a); gate[s] = rbf(1.f/(1.f+expf(-a))); }
}
__global__ void k_kgen(const u16* lar, const u16* aim, const u16* cr, const u16* ci, const u16* ldt, float* K){
  int d = blockIdx.y; int l = blockIdx.x*256+threadIdx.x;
  float dt = rbf(expf(bf2f(ldt[d])));
  float pos = rbf((float)l);
  float acc=0;
  for(int n=0;n<N;n++){
    float ar = -rbf(expf(bf2f(lar[d*N+n])))*dt, ai = bf2f(aim[d*N+n])*dt;
    float cre = bf2f(cr[d*N+n])*dt, cim = bf2f(ci[d*N+n])*dt;
    float e = expf(ar*pos); float sn, cs; sincosf(ai*pos,&sn,&cs);
    acc += cre*e*cs - cim*e*sn;
  }
  K[d*L+l] = 2.f*acc;
}
__global__ void k_conv(const u16* x, const float* K, const u16* dskip, float* a){
  int d = blockIdx.y; int l = blockIdx.x*256+threadIdx.x;
  float acc=0;
  for(int j=0;j<=l;j++) acc += K[d*L+j]*bf2f(x[(l-j)*D+d]);
  float y = rbf(acc);
  a[l*D+d] = rbf(y + rbf(bf2f(x[l*D+d])*bf2f(dskip[d])));
}
template<int MODE>
__global__ void k_gemm(const u16* A, const u16* B, int M, int Nn, int Kk, u16* out, const u16* z, const u16* bias, const float* gate, const float* a){
  __shared__ float sa[16][17], sb[16][17];
  int tx=threadIdx.x, ty=threadIdx.y; int m=blockIdx.y*16+ty, n=blockIdx.x*16+tx; float acc=0;
  for(int k0=0;k0<Kk;k0+=16){
    sa[ty][tx]=bf2f(A[(size_t)m*Kk+k0+tx]); sb[ty][tx]=bf2f(B[(size_t)(k0+ty)*Nn+n]); __syncthreads();
    for(int k=0;k<16;k++) acc += sa[ty][k]*sb[k][tx];
    __syncthreads();
  }
  if(MODE==0){ float v=rbf(acc); float t=tanhf(0.7978845608028654f*(v+0.044715f*v*v*v)); out[(size_t)m*Nn+n]=f2bf(0.5f*v*(1.f+t)); }
  if(MODE==1){ float g=rbf(rbf(acc)+bf2f(bias[m])); out[(size_t)m*Nn+n]=f2bf(bf2f(z[(size_t)m*F+n])*g); }
  if(MODE==2){ float b=rbf(acc); float g=gate[m]; out[(size_t)m*Nn+n]=f2bf(rbf(g*a[(size_t)m*Nn+n])+rbf(rbf(1.f-g)*b)); }
}
__global__ void k_ln(const u16* z, u16* z2){
  int s=blockIdx.x, lane=threadIdx.x; float a=0;
  for(int f=lane; f<FH; f+=64) a+=bf2f(z[(size_t)s*F+FH+f]);
  for(int o=32;o>=1;o>>=1) a += __shfl_xor(a,o);
  float m=a/FH; float q=0;
  for(int f=lane; f<FH; f+=64){ float d=bf2f(z[(size_t)s*F+FH+f])-m; q+=d*d; }
  for(int o=32;o>=1;o>>=1) q += __shfl_xor(q,o);
  float v=rbf(q/FH); m=rbf(m); float sd=rbf(sqrtf(rbf(v+1e-5f)));
  for(int f=lane; f<FH; f+=64) z2[(size_t)s*FH+f]=f2bf(rbf(bf2f(z[(size_t)s*F+FH+f])-m)/sd);
}
extern "C" void kernel_launch(void* const* d_in, const int* in_sizes, int n_in, void* d_out, int out_size, void* d_ws, size_t ws_size, hipStream_t stream){
  const u16* x=(const u16*)d_in[0]; const u16* lar=(const u16*)d_in[1]; const u16* aim=(const u16*)d_in[2]; const u16* cr=(const u16*)d_in[3]; const u16* ci=(const u16*)d_in[4];
  const u16* ldt=(const u16*)d_in[5]; const u16* dsk=(const u16*)d_in[6]; const u16* win=(const u16*)d_in[8]; const u16* wsp=(const u16*)d_in[9]; const u16* bsp=(const u16*)d_in[10]; const u16* wout=(const u16*)d_in[11]; const u16* gw=(const u16*)d_in[12];
  char* ws=(char*)d_ws;
  float* gate=(float*)ws; float* K=(float*)(ws+(1<<20)); float* a=(float*)(ws+(8<<20)); u16* z=(u16*)(ws+(16<<20)); u16* z2=(u16*)(ws+(40<<20)); u16* h=(u16*)(ws+(56<<20));
  k_gate<<<L,64,0,stream>>>(x,gw,gate);
  k_kgen<<<dim3(L/256,D),256,0,stream>>>(lar,aim,cr,ci,ldt,K);
  k_conv<<<dim3(L/256,D),256,0,stream>>>(x,K,dsk,a);
  k_gemm<0><<<dim3(F/16,L/16),dim3(16,16),0,stream>>>(x,win,L,F,D,z,nullptr,nullptr,nullptr,nullptr);
  k_ln<<<L,64,0,stream>>>(z,z2);
  k_gemm<1><<<dim3(FH/16,L/16),dim3(16,16),0,stream>>>(wsp,z2,L,FH,L,h,z,bsp,nullptr,nullptr);
  k_gemm<2><<<dim3(D/16,L/16),dim3(16,16),0,stream>>>(h,wout,L,D,FH,(u16*)d_out,nullptr,nullptr,gate,a);
}
```

```cpp
#include <hip/hip_runtime.h>
#include <hip/hip_bf16.h>

#define LAS __attribute__((address_space(3)))
#define GAS __attribute__((address_space(1)))
typedef unsigned short u16;
typedef unsigned u32x4 __attribute__((ext_vector_type(4)));
typedef unsigned u32x2 __attribute__((ext_vector_type(2)));
typedef short bf16x8 __attribute__((ext_vector_type(8)));
typedef short s16x4 __attribute__((ext_vector_type(4)));
typedef _Float16 f16x8 __attribute__((ext_vector_type(8)));
typedef _Float16 f16x2 __attribute__((ext_vector_type(2)));
typedef __bf16 bfx2 __attribute__((ext_vector_type(2)));
typedef float f32x4 __attribute__((ext_vector_type(4)));
typedef float f32x2 __attribute__((ext_vector_type(2)));

constexpr int SZ_F = 9437184;
constexpr int OFF_Z2 = 0, OFF_H = SZ_F, OFF_WF = 2 * SZ_F, OFF_XT = OFF_WF + 2654208, OFF_AT = OFF_XT + 2359296,
              OFF_GATE = OFF_AT + 2359296, OFF_ST = OFF_GATE + 8192, OFF_CTL = OFF_ST + 524288, WS_END = OFF_CTL + 32768;
constexpr int C_F2 = 1024, C_F3 = 2048, C_FW = 3072, C_FA = 3584;
constexpr int ZST = 40960, GST = 34816, L_PARK = 104448, L_PAR = 122880, L_KC = 141312, L_AT = 146688, L_SCR = 159744, LDS_BYTES = 163840;
constexpr int CV_W = 25856, CV_U = 18176, L_WPF = 103424, JL = 2;
constexpr unsigned SPIN_CAP = 1u << 22;

#define BAR() do { asm volatile("" ::: "memory"); __builtin_amdgcn_s_barrier(); asm volatile("" ::: "memory"); } while (0)
#define WAITV(n) asm volatile("s_waitcnt vmcnt(%0)" ::"i"(n) : "memory")
#define WAITL0() do { __builtin_amdgcn_s_waitcnt(0xC07F); asm volatile("" ::: "memory"); } while (0)
#define SB0() __builtin_amdgcn_sched_barrier(0)
#define RLX __ATOMIC_RELAXED, __HIP_MEMORY_SCOPE_AGENT
#define STAMP(k)
typedef __amdgpu_buffer_rsrc_t rsrc_t;
__device__ __forceinline__ rsrc_t mkrs(const void* p, int bytes) { return __builtin_amdgcn_make_buffer_rsrc((void*)p, 0, bytes, 0x00020000); }
__device__ __forceinline__ void dma16(rsrc_t r, LAS char* dst, int voff, int soff) { __builtin_amdgcn_raw_ptr_buffer_load_lds(r, (LAS void*)dst, 16, voff, soff, 0, 0); }
__device__ __forceinline__ void dma16c(rsrc_t r, LAS char* dst, int voff, int soff) { __builtin_amdgcn_raw_ptr_buffer_load_lds(r, (LAS void*)dst, 16, voff, soff, 0, 16); }
__device__ __forceinline__ unsigned pk2(float lo, float hi) { f32x2 v = {lo, hi}; return __builtin_bit_cast(unsigned, __builtin_convertvector(v, bfx2)); }
__device__ __forceinline__ float bflo(unsigned v) { return __uint_as_float(v << 16); }
__device__ __forceinline__ float bfhi(unsigned v) { return __uint_as_float(v & 0xffff0000u); }
__device__ __forceinline__ float bf2f(u16 v) { return __uint_as_float(((unsigned)v) << 16); }
__device__ __forceinline__ float rbf(float f) { return bflo(pk2(f, f)); }
__device__ __forceinline__ f32x2 gelu2(f32x2 v) {
  const f32x2 p = (v * v) * (-0.10294324f) + (-2.3022082f);
  const f32x2 a = v * p;
  f32x2 d; d.x = __builtin_amdgcn_exp2f(a.x); d.y = __builtin_amdgcn_exp2f(a.y);
  d = d + 1.0f;
  f32x2 r; r.x = __builtin_amdgcn_rcpf(d.x); r.y = __builtin_amdgcn_rcpf(d.y);
  return v * r;
}
template <int CTRL> __device__ __forceinline__ float dppadd(float x) { return x + __builtin_amdgcn_update_dpp(0.f, x, CTRL, 0xf, 0xf, true); }
template <int CTRL> __device__ __forceinline__ float dppmax(float x) { return fmaxf(x, __builtin_amdgcn_update_dpp(0.f, x, CTRL, 0xf, 0xf, true)); }
__device__ __forceinline__ float rowsum16(float x) { x = dppadd<0x128>(x); x = dppadd<0x124>(x); x = dppadd<0x122>(x); x = dppadd<0x121>(x); return x; }
__device__ __forceinline__ void wait_flags(const unsigned* f, bool act, unsigned E) {
  for (unsigned sp = 0; sp < SPIN_CAP; ++sp) {
    const unsigned v = __hip_atomic_load((GAS unsigned*)f, RLX);
    if (__all(!act || v == E)) break;
    __builtin_amdgcn_s_sleep(1);
  }
  asm volatile("" ::: "memory");
}
__device__ __forceinline__ void touch(unsigned& dummy, const void* p) { asm volatile("global_load_dword %0, %1, off" : "+v"(dummy) : "v"(p) : "memory"); }

struct ZF { bf16x8 x[2]; s16x4 w[9][2]; };

__global__ __launch_bounds__(384, 2) void gmix_fused(const u16* __restrict__ x, const u16* __restrict__ lar, const u16* __restrict__ aim, const u16* __restrict__ cre,
    const u16* __restrict__ cim, const u16* __restrict__ ldt, const u16* __restrict__ dsk, const u16* __restrict__ win, const u16* __restrict__ wsp,
    const u16* __restrict__ bsp, const u16* __restrict__ wout, const u16* __restrict__ gw, u16* __restrict__ out, char* ws
) {
  extern __shared__ __attribute__((aligned(16))) char smem_[];
  LAS char* smem = (LAS char*)smem_;
  const int wid = __builtin_amdgcn_readfirstlane(threadIdx.x >> 6), lane = threadIdx.x & 63;
  const int bx = blockIdx.x & 7, bl = blockIdx.x >> 3;
  const int tm = (bx >> 2) * 8 + (bl & 7), tn = (bx & 3) * 4 + (bl >> 3);
  const int pp = tn >> 1, ee = tn & 1;
  const int t3 = bx * 4 + (bl & 3), dn = bl >> 2;
  unsigned* ctl = (unsigned*)(ws + OFF_CTL);
  const unsigned E = 1u;
  STAMP(0);
  const int fr = lane & 15, fq = lane >> 4;
  const int v16 = lane * 16;

  if (wid >= 4) {
    const int lw = wid - 4;
    {
      const rsrc_t rX = mkrs(x + (size_t)tm * 128 * 576, 128 * 1152);
      const rsrc_t rWi = mkrs(win, 576 * 9216);
      const int sub = lane >> 3, ck = lane & 7;
      int vxP[2], vxN[2];
#pragma unroll
      for (int h = 0; h < 2; ++h) {
        const int swz = (ck ^ ((4 * h + (lane >> 4)) & 7)) << 4;
        const int i = 8 * h + sub;
        vxP[h] = (8 * (i >> 2) + (i & 3)) * 1152 + swz;
        vxN[h] = i * 1152 + swz;
      }
      int vw[6];
#pragma unroll
      for (int q = 0; q < 6; ++q) { const int o = q * 1024 + v16, row = o / 384, pos = (o - row * 384) >> 4; vw[q] = row * 9216 + ((pos ^ ((row & 2) | ((row & 8) >> 1))) << 4); }
      const int wcol = (288 * tn) & ~127;
      auto issueZ = [&](int g) {
        const bool z2 = g < 9; const int kt = z2 ? g : g - 9;
        LAS char* dst = smem + (g % 3) * ZST;
        const int sw = kt * 64 * 9216 + wcol + (z2 ? 4608 : 0);
        if (lw == 0) {
          const int v0 = z2 ? vxP[0] : vxN[0], v1 = z2 ? vxP[1] : vxN[1];
#pragma unroll
          for (int p = 0; p < 16; ++p) { const int tile = p >> 1; const int rowP = 32 * (tile >> 1) + 4 * (tile & 1), rowN = 16 * tile;
            dma16(rX, dst + p * 1024, (p & 1) ? v1 : v0, (z2 ? rowP : rowN) * 1152 + kt * 128); }
#pragma unroll
          for (int q = 0; q < 4; ++q) dma16(rWi, dst + 16384 + q * 1024, vw[q], sw);
        } else {
#pragma unroll
          for (int q = 4; q < 24; ++q) dma16(rWi, dst + 16384 + q * 1024, vw[q % 6], sw + (q / 6) * 16 * 9216);
        }
      };
      issueZ(0); issueZ(1); issueZ(2);
      WAITV(40); BAR();
#pragma unroll 1
      for (int g = 0; g < 15; ++g) { WAITV(20); BAR(); issueZ(g + 3); }
    }
    const rsrc_t rW = mkrs(wsp + (size_t)tm * 128 * 2048, 128 * 4096);
    const rsrc_t rZ = mkrs(ws + OFF_Z2, SZ_F);
    const int vW0 = (lane >> 3) * 4096 + (((lane & 7) ^ (lane >> 4)) << 4), vW1 = (lane >> 3) * 4096 + (((lane & 7) ^ (4 + (lane >> 4))) << 4);
    auto issueW = [&](int kt, int slot) {
      LAS char* dst = smem + slot * GST;
#pragma unroll
      for (int p = 0; p < 16; ++p) dma16(rW, dst + p * 1024, (p & 1) ? vW1 : vW0, p * 8 * 4096 + kt * 128);
    };
    auto issueZ2 = [&](int kt, int slot) {
      LAS char* dst = smem + slot * GST + 16384; const int so = (kt * 144 + 9 * tn) * 2048;
#pragma unroll
      for (int p = 0; p < 18; ++p) dma16c(rZ, dst + p * 1024, v16, so + p * 1024);
    };
    if (lw == 0) {
      WAITV(20); BAR(); issueW(0, 0);
      WAITV(16); BAR(); issueW(1, 1);
      BAR(); issueW(2, 2);
      WAITV(32); BAR();
      int slot = 0;
#pragma unroll 1
      for (int i = 0; i < 29; ++i) { WAITV(16); BAR(); issueW(i + 3, slot); slot = (slot == 2) ? 0 : slot + 1; }
      WAITV(16); BAR();
      WAITV(0); BAR();
      BAR();
    } else {
      WAITV(20); BAR();
      WAITV(0); BAR();
      BAR();
      wait_flags(ctl + C_F2 + tn * 64 + lane, true, E);
      STAMP(1);
      issueZ2(0, 0); issueZ2(1, 1); issueZ2(2, 2);
      WAITV(36); BAR();
      int slot = 0;
#pragma unroll 1
      for (int i = 0; i < 29; ++i) { WAITV(18); BAR(); issueZ2(i + 3, slot); slot = (slot == 2) ? 0 : slot + 1; }
      WAITV(18); BAR();
      WAITV(0); BAR();
      BAR();
    }
    BAR();
    BAR();
    BAR();
    if (lw == 0) {
      for (int k = 0; k < 5; ++k) wait_flags(ctl + C_FW + k * 64 + lane, k * 64 + lane < 288, E);
      const rsrc_t rF = mkrs(ws + OFF_WF + (size_t)dn * 72 * 4608, 72 * 4608);
#pragma unroll 4
      for (int p = 0; p < JL * 18; ++p) dma16c(rF, smem + L_WPF + p * 1024, v16, p * 1024);
      WAITV(0);
    } else {
      wait_flags(ctl + C_F3 + (t3 >> 1) * 64 + lane, true, E);
      STAMP(2);
    }
    BAR();
    if (lw == 0) {
      wait_flags(ctl + C_FA + dn * 128 + lane, true, E);
      wait_flags(ctl + C_FA + dn * 128 + 64 + lane, lane < 8, E);
      const rsrc_t rA = mkrs(ws + OFF_AT + (size_t)dn * 72 * 4096, 72 * 4096);
#pragma unroll
      for (int p = 0; p < 9; ++p) dma16c(rA, smem + L_AT + p * 1024, (lane >> 3) * 4096 + (lane & 7) * 16, p * 8 * 4096 + t3 * 128);
      const rsrc_t rG = mkrs(ws + OFF_GATE + t3 * 256, 256);
      dma16c(rG, smem + L_AT + 9216, v16, 0);
      WAITV(0);
    }
    BAR();
    BAR();
    return;
  }

  const bool ssm = tm < 9;
  const int uP = (tm - 9) * 16 + tn;
  const bool retile = !ssm && uP < 72;
  const int dch = 36 * tn + 4 * tm + wid;
  LAS char* scr = smem + L_SCR + wid * 512;
  unsigned dummy = 0;
  u16 pl[2][4]; u16 pdt = 0, pds = 0; u32x4 wsrc[9];
  if (ssm) {
#pragma unroll
    for (int h = 0; h < 2; ++h) { const int o = dch * 128 + h * 64 + lane; pl[h][0] = lar[o]; pl[h][1] = aim[o]; pl[h][2] = cre[o]; pl[h][3] = cim[o]; }
    pdt = ldt[dch]; pds = dsk[dch];
  }
  const int grow = tm * 128 + 8 * tn + 2 * wid;
  u32x4 gx[3], gg[3];
#pragma unroll
  for (int k = 0; k < 3; ++k) { int c = k * 64 + lane; c = c < 144 ? c : 143; gx[k] = *(const u32x4*)(x + (size_t)grow * 576 + c * 8); gg[k] = *(const u32x4*)(gw + (c >= 72 ? c - 72 : c) * 8); }
  float bb[2];
#pragma unroll
  for (int u = 0; u < 2; ++u) bb[u] = bf2f(bsp[(tm * 8 + 2 * wid + u) * 16 + fr]);
  if (retile) {
    const int p9 = uP / 9, r9 = uP - p9 * 9;
#pragma unroll
    for (int k = 0; k < 9; ++k) { const int c = k * 64 + lane, row = c / 18, ch = c - row * 18;
      const int f = (r9 < 8) ? 144 * (2 * p9 + (r9 >> 2)) + 32 * (r9 & 3) + row : 144 * (2 * p9 + (row >> 4)) + 128 + (row & 15);
      wsrc[k] = *(const u32x4*)(wout + (size_t)f * 576 + wid * 144 + ch * 8); }
  }
  {
    const char* wv = (const char*)win + (size_t)((bl & 7) * 48 + wid * 12 + (lane >> 2)) * 9216 + ((288 * tn) & ~127) + (lane & 3) * 128;
    if ((lane >> 2) < 12 && (lane & 3) < 3) { touch(dummy, wv + 192 * 9216 + 4608); touch(dummy, wv + 192 * 9216); }
    const char* xv = (const char*)x + (size_t)(tm * 128 + (bl >> 3) * 32 + wid * 8 + (lane >> 3)) * 1152 + 384 + (lane & 7) * 128;
    if ((lane & 7) < 6) touch(dummy, xv);
    touch(dummy, (const char*)wsp + (size_t)(blockIdx.x * 4 + wid) * 8192 + lane * 128);
  }
  asm volatile("" : "+v"(bb[0]), "+v"(bb[1]));
  {
    float s0 = 0.f, s1 = 0.f;
#pragma unroll
    for (int k = 0; k < 3; ++k) { float s = 0.f;
#pragma unroll
      for (int j = 0; j < 4; ++j) s += bflo(gx[k][j]) * bflo(gg[k][j]) + bfhi(gx[k][j]) * bfhi(gg[k][j]);
      const int c = k * 64 + lane; s0 += (c < 72) ? s : 0.f; s1 += (c >= 72 && c < 144) ? s : 0.f; }
    s0 = rowsum16(s0); s1 = rowsum16(s1);
    s0 += __shfl_xor(s0, 16); s1 += __shfl_xor(s1, 16); s0 += __shfl_xor(s0, 32); s1 += __shfl_xor(s1, 32);
    const float sv = lane ? s1 : s0;
    const float gv = __builtin_amdgcn_rcpf(1.f + __builtin_amdgcn_exp2f(-1.44269504f * sv));
    if (lane < 2) __hip_atomic_store((GAS unsigned*)(ws + OFF_GATE) + grow + lane, __float_as_uint(gv), RLX);
  }
  const float dskf = __uint_as_float(__builtin_amdgcn_readfirstlane(((unsigned)pds) << 16));
  if (ssm) {
    const float dt = rbf(__expf(bf2f(pdt)));
    float mx = 0.f;
#pragma unroll
    for (int h = 0; h < 2; ++h) mx = fmaxf(mx, fmaxf(fabsf(bf2f(pl[h][2])), fabsf(bf2f(pl[h][3]))));
    mx = dppmax<0x128>(mx); mx = dppmax<0x124>(mx); mx = dppmax<0x122>(mx); mx = dppmax<0x121>(mx);
    mx = fmaxf(mx, __shfl_xor(mx, 16)); mx = fmaxf(mx, __shfl_xor(mx, 32));
    const int ex = (mx > 0.f && mx < 3.0e38f) ? __builtin_amdgcn_frexp_expf(mx) : 0;
    LAS char* par = smem + L_PAR + wid * 2048;
#pragma unroll
    for (int h = 0; h < 2; ++h) {
      const float ar = -rbf(__expf(bf2f(pl[h][0]))) * dt, ai = bf2f(pl[h][1]) * dt;
      f32x4 pv = {ar * 1.44269504f, ai * 0.15915494f, __builtin_amdgcn_ldexpf(bf2f(pl[h][2]), -ex), __builtin_amdgcn_ldexpf(bf2f(pl[h][3]), -ex)};
      *(LAS f32x4*)(par + (h * 64 + lane) * 16) = pv;
    }
    float hp[4];
    hp[0] = (float)(128 * (fr >> 3) + (fr & 7));
    hp[1] = (float)(fr < 8 ? 256 + 128 * (fr >> 2) + 2 * (fr & 3) : 512 + 128 * ((fr - 8) >> 1) + 4 * (fr & 1));
    hp[2] = (float)(fr < 8 ? 1024 + 128 * fr : 2048);
    hp[3] = (float)(8 * fr);
    f32x4 kd[3] = {{0, 0, 0, 0}, {0, 0, 0, 0}, {0, 0, 0, 0}};
#pragma unroll 1
    for (int kp = 0; kp < 8; ++kp) {
      u32x4 fa[4];
#pragma unroll
      for (int m = 0; m < 4; ++m) {
        const f32x4 pv = *(LAS f32x4*)(par + (kp * 16 + fq * 4 + m) * 16);
#pragma unroll
        for (int t = 0; t < 4; ++t) {
          const float e = __builtin_amdgcn_exp2f(pv[0] * hp[t]);
          const float ph = __builtin_amdgcn_fractf(pv[1] * hp[t]);
          float xr = e * __builtin_amdgcn_cosf(ph), xi = e * __builtin_amdgcn_sinf(ph);
          if (t == 3) { const float yr = pv[2] * xr - pv[3] * xi, yi = pv[2] * xi + pv[3] * xr; xr = yr; xi = -yi; }
          f32x2 v = {xr, xi}; fa[t][m] = __builtin_bit_cast(unsigned, __builtin_convertvector(v, f16x2));
        }
      }
#pragma unroll
      for (int t = 0; t < 3; ++t) kd[t] = __builtin_amdgcn_mfma_f32_16x16x32_f16(__builtin_bit_cast(f16x8, fa[t]), __builtin_bit_cast(f16x8, fa[3]), kd[t], 0, 0, 0);
    }
    const float ksc = __builtin_amdgcn_ldexpf(2.f * dt, ex);
    LAS u16* kc = (LAS u16*)(smem + L_KC + wid * 1344);
#pragma unroll
    for (int j = 0; j < 4; ++j) { const int r = fq * 4 + j;
      kc[128 * (r >> 3) + (r & 7) + 8 * fr] = (u16)pk2(kd[0][j] * ksc, 0.f);
      kc[r < 8 ? 256 + 64 * (r >> 2) + (r & 3) + 4 * fr : 384 + 32 * ((r - 8) >> 1) + (r & 1) + 2 * fr] = (u16)pk2(kd[1][j] * ksc, 0.f);
      if (r < 8 || (r == 8 && fr == 0)) kc[512 + 16 * r + fr] = (u16)pk2(kd[2][j] * ksc, 0.f); }
  }
  STAMP(1);
  const int xo0 = (32 * wid + fr) * 128 + ((fq ^ ((fr >> 1) & 7)) << 4), xo1 = (32 * wid + fr) * 128 + (((4 + fq) ^ ((fr >> 1) & 7)) << 4);
  int wo[4];
  { const int q4 = fr >> 2, p4 = lane & 3, cb = ((288 * tn) & 127) >> 4, sz = (q4 & 2) | ((fq & 1) << 2);
#pragma unroll
    for (int a = 0; a < 4; ++a) wo[a] = 16384 + (8 * fq + q4) * 384 + (((cb + 2 * a + (p4 >> 1)) ^ sz) << 4) + 8 * (p4 & 1); }
  auto rdz = [&](ZF& F, int sl, int kk) {
    LAS char* s = smem + sl;
#pragma unroll
    for (int e = 0; e < 2; ++e) F.x[e] = *(LAS bf16x8*)(s + (kk ? xo1 : xo0) + e * 2048);
#pragma unroll
    for (int a = 0; a < 9; ++a)
#pragma unroll
      for (int t = 0; t < 2; ++t) F.w[a][t] = __builtin_amdgcn_ds_read_tr16_b64_v4i16((LAS s16x4*)(s + wo[a & 3] + (a >> 2) * 128 + (32 * kk + 4 * t) * 384));
  };
  f32x4 za[2][9];
  auto zzero = [&]() {
#pragma unroll
    for (int e = 0; e < 2; ++e)
#pragma unroll
      for (int a = 0; a < 9; ++a) za[e][a] = (f32x4){0, 0, 0, 0}; };
  auto mmz = [&](const ZF& F, bool z2) {
#pragma unroll
    for (int a = 0; a < 9; ++a) { const bf16x8 wv = __builtin_shufflevector(F.w[a][0], F.w[a][1], 0, 1, 2, 3, 4, 5, 6, 7);
#pragma unroll
      for (int e = 0; e < 2; ++e) za[e][a] = z2 ? __builtin_amdgcn_mfma_f32_16x16x32_bf16(F.x[e], wv, za[e][a], 0, 0, 0) : __builtin_amdgcn_mfma_f32_16x16x32_bf16(wv, F.x[e], za[e][a], 0, 0, 0); }
  };
  auto schz = [&]() {
#pragma unroll
    for (int i = 0; i < 18; ++i) { __builtin_amdgcn_sched_group_barrier(0x8, 1, 0); __builtin_amdgcn_sched_group_barrier(0x100, 1, 0); }
    __builtin_amdgcn_sched_group_barrier(0x100, 2, 0); SB0(); };
  ZF F0, F1;
  zzero();
  BAR();
  STAMP(2);
  rdz(F0, 0, 0);
  const rsrc_t rXT = mkrs(ws + OFF_XT + (size_t)(tn * 64 + tm * 4 + wid) * 2304, 2304);
  {
    int sl = 0;
#pragma unroll 1
    for (int g = 0; g < 9; ++g) {
      rdz(F1, sl, 1); mmz(F0, true); schz();
      if (g == (9 * tn) >> 4 || g == (9 * tn + 8) >> 4) {
        const int q4 = fr >> 2, p4 = lane & 3;
#pragma unroll
        for (int ct = 0; ct < 3; ++ct) {
          const int lq = (9 * tn + 4 * ct + p4) & 15; s16x4 h[2];
#pragma unroll
          for (int e = 0; e < 2; ++e) { const int rho = 32 * wid + 16 * e + 4 * fq + q4;
            h[e] = __builtin_amdgcn_ds_read_tr16_b64_v4i16((LAS s16x4*)(smem + sl + rho * 128 + (((lq >> 1) ^ ((rho >> 1) & 7)) << 4) + 8 * (lq & 1))); }
          const int myq = 4 * ct + (fr >> 2);
          if (myq < 9 && ((9 * tn + myq) >> 4) == g)
            __builtin_amdgcn_raw_buffer_store_b128(__builtin_bit_cast(u32x4, __builtin_shufflevector(h[0], h[1], 0, 1, 2, 3, 4, 5, 6, 7)), rXT, (16 * ct + fr) * 64 + fq * 16, 0, 16);
        }
      }
      WAITL0(); BAR();
      sl = (sl == 2 * ZST) ? 0 : sl + ZST;
      rdz(F0, sl, 0); mmz(F1, true); schz();
    }
  }
  STAMP(3);
  u32x4 zr[9];
  {
    f32x2 S[4], Q[4];
#pragma unroll
    for (int k = 0; k < 4; ++k) { S[k] = (f32x2){0, 0}; Q[k] = (f32x2){0, 0}; }
#pragma unroll
    for (int a = 0; a < 9; ++a)
#pragma unroll
      for (int k = 0; k < 4; ++k) { const f32x4 c = za[k >> 1][a]; f32x2 v = (k & 1) ? (f32x2){c[2], c[3]} : (f32x2){c[0], c[1]};
        v = gelu2(v); S[k] += v; Q[k] += v * v; zr[a][k] = pk2(v.x, v.y); }
#pragma unroll
    for (int k = 0; k < 4; ++k) { S[k].x = rowsum16(S[k].x); S[k].y = rowsum16(S[k].y); Q[k].x = rowsum16(Q[k].x); Q[k].y = rowsum16(Q[k].y); }
    if (fr == 0) {
      LAS f32x4* d = (LAS f32x4*)(scr + fq * 64);
      d[0] = (f32x4){S[0].x, S[0].y, S[1].x, S[1].y}; d[1] = (f32x4){S[2].x, S[2].y, S[3].x, S[3].y};
      d[2] = (f32x4){Q[0].x, Q[0].y, Q[1].x, Q[1].y}; d[3] = (f32x4){Q[2].x, Q[2].y, Q[3].x, Q[3].y};
    }
    const unsigned val = *(LAS unsigned*)(scr + lane * 4);
    GAS unsigned long long* gp = (GAS unsigned long long*)(ws + OFF_ST + ((tm * 16 + tn) * 128 + 32 * wid + 8 * fq + (fr & 7)) * 16 + (fr >> 3) * 8);
    __hip_atomic_store(gp, ((unsigned long long)E << 32) | val, RLX);
  }
  STAMP(4);
  if (retile) {
    LAS char* rs = smem + L_PAR + wid * 9216;
#pragma unroll
    for (int k = 0; k < 9; ++k) *(LAS u32x4*)(rs + (k * 64 + lane) * 16) = wsrc[k];
    const bool nat = (uP % 9) == 8; const int q4 = fr >> 2, p4 = lane & 3;
    const rsrc_t rF = mkrs(ws + OFF_WF, 2654208);
#pragma unroll
    for (int dl = 0; dl < 2; ++dl)
#pragma unroll
      for (int c = 0; c < 5; ++c) { s16x4 h[2];
#pragma unroll
        for (int t = 0; t < 2; ++t) { const int row = nat ? 8 * fq + 4 * t + q4 : 16 * t + 4 * fq + q4; int col = 72 * dl + 16 * c + 4 * p4; col = col > 140 ? 140 : col;
          h[t] = __builtin_amdgcn_ds_read_tr16_b64_v4i16((LAS s16x4*)(rs + row * 288 + col * 2)); }
        const u32x4 v = __builtin_bit_cast(u32x4, __builtin_shufflevector(h[0], h[1], 0, 1, 2, 3, 4, 5, 6, 7));
        const int so = ((2 * wid + dl) * 72 + uP) * 4608 + c * 1024;
        if (c < 4) __builtin_amdgcn_raw_buffer_store_b128(v, rF, v16, so, 16);
        else if (fr < 8) __builtin_amdgcn_raw_buffer_store_b128(v, rF, (fq * 8 + fr) * 16, so, 16);
      }
  }
  {
    const rsrc_t rS = mkrs(ws + OFF_ST + (size_t)tm * 16 * 2048, 16 * 2048);
    float s = 0.f, q = 0.f;
    for (unsigned sp = 0; sp < SPIN_CAP; ++sp) {
      u32x4 gr[8]; bool ok = true; s = 0.f; q = 0.f;
#pragma unroll
      for (int k = 0; k < 8; ++k) gr[k] = __builtin_amdgcn_raw_buffer_load_b128(rS, ((lane >> 5) * 8 * 128 + 32 * wid + (lane & 31)) * 16, k * 2048, 16);
#pragma unroll
      for (int k = 0; k < 8; ++k) { ok = ok && gr[k][1] == E && gr[k][3] == E; s += __uint_as_float(gr[k][0]); q += __uint_as_float(gr[k][2]); }
      if (__all(ok)) break;
      __builtin_amdgcn_s_sleep(1);
    }
    s += __shfl_xor(s, 32); q += __shfl_xor(q, 32);
    const float mean = s * (1.f / 2304.f), var = fmaxf(q * (1.f / 2304.f) - mean * mean, 0.f), rs = __builtin_amdgcn_rsqf(var + 1e-5f);
    if (lane < 32) *(LAS f32x2*)(scr + lane * 8) = (f32x2){rs, -mean * rs};
    f32x4 rn[4];
#pragma unroll
    for (int k = 0; k < 4; ++k) rn[k] = *(LAS f32x4*)(scr + fq * 64 + k * 16);
    const rsrc_t rZs = mkrs(ws + OFF_Z2, SZ_F);
    const int zo = (((2 * tm + (wid >> 1)) * 144 + 9 * tn) * 2 + (wid & 1)) * 1024;
#pragma unroll
    for (int a = 0; a < 9; ++a) { u32x4 o;
#pragma unroll
      for (int k = 0; k < 4; ++k) o[k] = pk2(bflo(zr[a][k]) * rn[k][0] + rn[k][1], bfhi(zr[a][k]) * rn[k][2] + rn[k][3]);
      __builtin_amdgcn_raw_buffer_store_b128(o, rZs, v16, zo + a * 2048, 16); }
  }
  STAMP(5);
  zzero();
  {
    int sl = 0;
#pragma unroll 1
    for (int g = 0; g < 9; ++g) {
      rdz(F1, sl, 1); mmz(F0, false); schz();
      if (g == 6) {
        WAITV(0);
        if (lane == 0) { __hip_atomic_store((GAS unsigned*)ctl + C_F2 + tn * 64 + tm * 4 + wid, E, RLX);
          if (retile) __hip_atomic_store((GAS unsigned*)ctl + C_FW + uP * 4 + wid, E, RLX); }
      }
      WAITL0(); BAR();
      sl = (sl == 2 * ZST) ? 0 : sl + ZST;
      rdz(F0, sl, 0); mmz(F1, false); schz();
    }
  }
  asm volatile("" ::"v"(dummy));
  STAMP(6);
  {
    LAS char* pk = smem + L_PARK + wid * 9216 + v16;
    unsigned g8[2][2];
#pragma unroll
    for (int u = 0; u < 2; ++u) {
#pragma unroll
      for (int c = 0; c < 4; ++c) { u32x4 o;
#pragma unroll
        for (int k = 0; k < 4; ++k) { const f32x4 cc = za[u][2 * c + (k >> 1)]; f32x2 v = (k & 1) ? (f32x2){cc[2], cc[3]} : (f32x2){cc[0], cc[1]}; v = gelu2(v); o[k] = pk2(v.x, v.y); }
        *(LAS u32x4*)(pk + (u * 4 + c) * 1024) = o; }
#pragma unroll
      for (int k = 0; k < 2; ++k) { const f32x4 cc = za[u][8]; f32x2 v = k ? (f32x2){cc[2], cc[3]} : (f32x2){cc[0], cc[1]}; v = gelu2(v); g8[u][k] = pk2(v.x, v.y); }
    }
    *(LAS u32x4*)(pk + 8192) = (u32x4){g8[0][0], g8[0][1], g8[1][0], g8[1][1]};
    *(LAS f32x2*)(scr + lane * 8) = (f32x2){bb[0], bb[1]};
  }
  STAMP(7);
  const int kh = wid & 1, th = wid >> 1;
  f32x4 keep[9][2];
  u32x4 ul[4];
  {
    const int zoff = 16384 + kh * 1024 + v16;
    const int woff = (th * 64 + fr) * 128 + (((kh * 4 + fq) ^ ((fr >> 1) & 7)) << 4);
    f32x4 acc[9][4];
#pragma unroll
    for (int a = 0; a < 9; ++a)
#pragma unroll
      for (int b = 0; b < 4; ++b) acc[a][b] = (f32x4){0, 0, 0, 0};
    bf16x8 Z0[9], W0[4], Z1[9], W1[4];
    auto rd = [&](bf16x8(&Z)[9], bf16x8(&W)[4], int sl) {
      LAS char* s = smem + sl;
#pragma unroll
      for (int b = 0; b < 4; ++b) W[b] = *(LAS bf16x8*)(s + woff + b * 2048);
#pragma unroll
      for (int a = 0; a < 9; ++a) Z[a] = *(LAS bf16x8*)(s + zoff + a * 2048);
    };
    auto mm = [&](bf16x8(&Z)[9], bf16x8(&W)[4]) {
#pragma unroll
      for (int a = 0; a < 9; ++a)
#pragma unroll
        for (int b = 0; b < 4; ++b) acc[a][b] = __builtin_amdgcn_mfma_f32_16x16x32_bf16(Z[a], W[b], acc[a][b], 0, 0, 0);
    };
    auto sched = [&]() {
#pragma unroll
      for (int i = 0; i < 13; ++i) { __builtin_amdgcn_sched_group_barrier(0x8, 2, 0); __builtin_amdgcn_sched_group_barrier(0x100, 1, 0); }
      __builtin_amdgcn_sched_group_barrier(0x8, 10, 0); SB0();
    };
    WAITL0(); BAR();
    STAMP(8);
    rd(Z0, W0, 0);
    int sl = 0;
#pragma unroll 1
    for (int i = 0; i < 32; i += 2) {
      sl = (sl == 2 * GST) ? 0 : sl + GST;
      WAITL0(); BAR();
      rd(Z1, W1, sl); mm(Z0, W0); sched();
      sl = (sl == 2 * GST) ? 0 : sl + GST;
      WAITL0(); BAR();
      rd(Z0, W0, sl); mm(Z1, W1); sched();
    }
    STAMP(9);
    if (ssm) {
      const rsrc_t rU = mkrs(ws + OFF_XT + (size_t)tn * 64 * 2304, 64 * 2304);
#pragma unroll
      for (int k = 0; k < 4; ++k) ul[k] = __builtin_amdgcn_raw_buffer_load_b128(rU, lane * 2304 + k * 16, (4 * tm + wid) * 64, 16);
    }
    WAITL0(); BAR();
    LAS char* dst = smem + wid * 18432 + v16;
    if (kh) {
#pragma unroll
      for (int a = 0; a < 9; ++a)
#pragma unroll
        for (int u = 0; u < 2; ++u) { *(LAS f32x4*)(dst + (a * 2 + u) * 1024) = acc[a][u]; keep[a][u] = acc[a][2 + u]; }
    } else {
#pragma unroll
      for (int a = 0; a < 9; ++a)
#pragma unroll
        for (int u = 0; u < 2; ++u) { *(LAS f32x4*)(dst + (a * 2 + u) * 1024) = acc[a][2 + u]; keep[a][u] = acc[a][u]; }
    }
  }
  WAITL0(); BAR();
  {
    const rsrc_t rHs = mkrs(ws + OFF_H, SZ_F);
    LAS char* src = smem + (wid ^ 1) * 18432 + v16;
    LAS char* pk = smem + L_PARK + wid * 9216 + v16;
    const u32x4 z8 = *(LAS u32x4*)(pk + 8192);
    const f32x2 bq = *(LAS f32x2*)(scr + lane * 8);
#pragma unroll
    for (int u = 0; u < 2; ++u) {
      f32x4 g[9];
#pragma unroll
      for (int a = 0; a < 9; ++a) g[a] = keep[a][u] + *(LAS f32x4*)(src + (a * 2 + u) * 1024) + (u ? bq.y : bq.x);
      const int tb = tm * 8 + 2 * wid + u, ho = (tb * 72 + 9 * pp + 4 * ee) * 1024;
#pragma unroll
      for (int c = 0; c < 4; ++c) { const u32x4 z = *(LAS u32x4*)(pk + (u * 4 + c) * 1024); u32x4 o;
        o[0] = pk2(bflo(z[0]) * g[2 * c][0], bfhi(z[0]) * g[2 * c][1]); o[1] = pk2(bflo(z[1]) * g[2 * c][2], bfhi(z[1]) * g[2 * c][3]);
        o[2] = pk2(bflo(z[2]) * g[2 * c + 1][0], bfhi(z[2]) * g[2 * c + 1][1]); o[3] = pk2(bflo(z[3]) * g[2 * c + 1][2], bfhi(z[3]) * g[2 * c + 1][3]);
        __builtin_amdgcn_raw_buffer_store_b128(o, rHs, v16, ho + c * 1024, 16); }
      u32x2 o; o[0] = pk2(bflo(z8[2 * u]) * g[8][0], bfhi(z8[2 * u]) * g[8][1]); o[1] = pk2(bflo(z8[2 * u + 1]) * g[8][2], bfhi(z8[2 * u + 1]) * g[8][3]);
      __builtin_amdgcn_raw_buffer_store_b64(o, rHs, ee * 512 + (fr + 16 * (fq >> 1)) * 16 + (fq & 1) * 8, (tb * 72 + 9 * pp + 8) * 1024, 16);
    }
  }
  WAITL0(); BAR();
  STAMP(10);
  if (ssm) {
    LAS char* cv = smem + wid * CV_W;
    LAS char* ub = cv + CV_U;
    {
#pragma unroll
      for (int k = 0; k < 4; ++k) { const u32x4 s = ul[3 - k]; u32x4 o;
#pragma unroll
        for (int m = 0; m < 4; ++m) o[m] = __builtin_amdgcn_alignbit(s[3 - m], s[3 - m], 16);
        *(LAS u32x4*)(ub + (15 + lane) * 96 + k * 16) = o;
        if (lane < 15) *(LAS u32x4*)(ub + lane * 96 + k * 16) = (u32x4){0, 0, 0, 0}; }
    }
    const int cb0 = 248, cb1 = 4608 + 8, cb2 = 2 * 4608 + 128, cb3 = 3 * 4608 + 136;
    {
      LAS u16* kc = (LAS u16*)(smem + L_KC + wid * 1344);
      const int r = lane < 8 ? 0 : lane < 16 ? 1 : lane < 32 ? 2 : 3, bias = r ? (1 << (r - 1)) - 1 : 0, wd = r ? 1 : 0;
#pragma unroll
      for (int k = 0; k < 8; ++k) { u32x2 o;
#pragma unroll
        for (int m = 0; m < 2; ++m) { unsigned e2[2];
#pragma unroll
          for (int n = 0; n < 2; ++n) { const int j = 32 * lane + 4 * k + 2 * m + n; e2[n] = kc[128 * r + ((j + bias + (int)__builtin_amdgcn_ubfe(j, r, wd)) >> r)]; }
          o[m] = e2[0] | (e2[1] << 16); }
        *(LAS u32x2*)(cv + cb0 + 64 + 64 * lane + 8 * k) = o; }
      if (lane < 8) *(LAS u32x2*)(cv + cb0 + 8 * lane) = (u32x2){0, 0};
      if (lane == 8) *(LAS u32x2*)(cv + cb0 + 64 + 4096) = (u32x2){0, 0};
    }
    WAITV(0);
    if (lane == 0) __hip_atomic_store((GAS unsigned*)ctl + C_F3 + tm * 64 + tn * 4 + wid, E, RLX);
    STAMP(11);
    {
      unsigned d[18];
#pragma unroll
      for (int k = 0; k < 9; ++k) { const u32x2 v = *(LAS u32x2*)(cv + cb0 + 64 + 64 * lane + 8 * k); d[2 * k] = v[0]; d[2 * k + 1] = v[1]; }
#pragma unroll
      for (int k = 0; k < 8; ++k) {
        *(LAS u32x2*)(cv + cb1 + 64 + 64 * lane + 8 * k) = (u32x2){__builtin_amdgcn_alignbit(d[2 * k + 1], d[2 * k], 16), __builtin_amdgcn_alignbit(d[2 * k + 2], d[2 * k + 1], 16)};
        *(LAS u32x2*)(cv + cb2 + 64 + 64 * lane + 8 * k) = (u32x2){d[2 * k + 1], d[2 * k + 2]};
        *(LAS u32x2*)(cv + cb3 + 64 + 64 * lane + 8 * k) = (u32x2){__builtin_amdgcn_alignbit(d[2 * k + 2], d[2 * k + 1], 16), __builtin_amdgcn_alignbit(d[2 * k + 3], d[2 * k + 2], 16)};
      }
      const unsigned k01 = *(LAS unsigned*)(cv + cb0 + 64), k23 = *(LAS unsigned*)(cv + cb0 + 68);
      if (lane < 8) { const bool l7 = lane == 7;
        *(LAS u32x2*)(cv + cb1 + 8 * lane) = (u32x2){0u, l7 ? (k01 << 16) : 0u};
        *(LAS u32x2*)(cv + cb2 + 8 * lane) = (u32x2){0u, l7 ? k01 : 0u};
        *(LAS u32x2*)(cv + cb3 + 8 * lane) = (u32x2){l7 ? (k01 << 16) : 0u, l7 ? __builtin_amdgcn_alignbit(k23, k01, 16) : 0u}; }
    }
    const int sg = (fr + 1) & 3;
    int aA = (sg == 0 ? cb0 : sg == 1 ? cb1 : sg == 2 ? cb2 : cb3) + 2 * (8 * (fr >> 2) + 8 * fq + (((fr & 3) + 1) & 4));
    int aB = CV_U + 96 * (15 + fr) + 16 * fq;
    f32x4 ya[4][2];
#pragma unroll
    for (int c = 0; c < 4; ++c) { ya[c][0] = (f32x4){0, 0, 0, 0}; ya[c][1] = (f32x4){0, 0, 0, 0}; }
#pragma unroll
    for (int era = 0; era < 4; ++era) {
#pragma unroll 4
      for (int k = 0; k < 16; ++k) {
        int a1 = aA + 8, a2 = aA + 16; asm volatile("" : "+v"(a1), "+v"(a2));
        const u32x2 r0 = *(LAS u32x2*)(cv + aA), r1 = *(LAS u32x2*)(cv + a1), r2 = *(LAS u32x2*)(cv + a2);
        const bf16x8 A0 = __builtin_bit_cast(bf16x8, (u32x4){r0[0], r0[1], r1[0], r1[1]}), A1 = __builtin_bit_cast(bf16x8, (u32x4){r1[0], r1[1], r2[0], r2[1]});
#pragma unroll
        for (int c = era; c < 4; ++c) { const bf16x8 B = *(LAS bf16x8*)(cv + aB + 1536 * c);
          ya[c][0] = __builtin_amdgcn_mfma_f32_16x16x32_bf16(A0, B, ya[c][0], 0, 0, 0);
          ya[c][1] = __builtin_amdgcn_mfma_f32_16x16x32_bf16(A1, B, ya[c][1], 0, 0, 0); }
        aA += 64; aB -= 96;
      }
    }
    const rsrc_t rA = mkrs(ws + OFF_AT + (size_t)dch * 4096, 4096);
#pragma unroll
    for (int c = 0; c < 4; ++c) { const u32x4 uu = *(LAS u32x4*)(ub + 96 * (15 + 16 * c + fr) + 16 * (3 - fq)); u32x4 o;
      o[0] = pk2(ya[c][0][0] + bfhi(uu[3]) * dskf, ya[c][0][1] + bflo(uu[3]) * dskf); o[1] = pk2(ya[c][0][2] + bfhi(uu[2]) * dskf, ya[c][0][3] + bflo(uu[2]) * dskf);
      o[2] = pk2(ya[c][1][0] + bfhi(uu[1]) * dskf, ya[c][1][1] + bflo(uu[1]) * dskf); o[3] = pk2(ya[c][1][2] + bfhi(uu[0]) * dskf, ya[c][1][3] + bflo(uu[0]) * dskf);
      __builtin_amdgcn_raw_buffer_store_b128(o, rA, fr * 64 + fq * 16, c * 1024, 16); }
  } else {
    WAITV(0);
    if (lane == 0) __hip_atomic_store((GAS unsigned*)ctl + C_F3 + tm * 64 + tn * 4 + wid, E, RLX);
    STAMP(11);
  }
  STAMP(12);
  constexpr int DEPTH = 3;
  {
    const rsrc_t rH = mkrs(ws + OFF_H + (size_t)t3 * 4 * 72 * 1024, 4 * 72 * 1024);
    const rsrc_t rF = mkrs(ws + OFF_WF + (size_t)dn * 72 * 4608, 72 * 4608);
    const int vh = 4096 + (fq * 8 + (fr & 7)) * 16;
    bf16x8 H[DEPTH][4], W[DEPTH][5];
    auto ldH = [&](int s, int j) { const int P = j * 4 + wid;
#pragma unroll
      for (int b = 0; b < 4; ++b) H[s][b] = __builtin_bit_cast(bf16x8, __builtin_amdgcn_raw_buffer_load_b128(rH, v16, P * 1024 + b * 72 * 1024, 16)); };
    auto ldW = [&](int s, int j) { const int P = j * 4 + wid;
      if (j < JL) { LAS char* p = smem + L_WPF + P * 4608;
#pragma unroll
        for (int c = 0; c < 4; ++c) W[s][c] = *(LAS bf16x8*)(p + v16 + c * 1024);
        W[s][4] = *(LAS bf16x8*)(p + vh);
      } else {
#pragma unroll
        for (int c = 0; c < 4; ++c) W[s][c] = __builtin_bit_cast(bf16x8, __builtin_amdgcn_raw_buffer_load_b128(rF, v16 + c * 1024, P * 4608, 16));
        W[s][4] = __builtin_bit_cast(bf16x8, __builtin_amdgcn_raw_buffer_load_b128(rF, vh, P * 4608, 16));
      } };
    BAR();
    STAMP(13);
#pragma unroll
    for (int s = 0; s < DEPTH; ++s) { ldH(s, s); ldW(s, s); }
    f32x4 ac3[4][5];
#pragma unroll
    for (int b = 0; b < 4; ++b)
#pragma unroll
      for (int c = 0; c < 5; ++c) ac3[b][c] = (f32x4){0, 0, 0, 0};
#pragma unroll
    for (int j = 0; j < 18; ++j) {
      const int s = j % DEPTH;
#pragma unroll
      for (int b = 0; b < 4; ++b)
#pragma unroll
        for (int c = 0; c < 5; ++c) ac3[b][c] = __builtin_amdgcn_mfma_f32_16x16x32_bf16(H[s][b], W[s][c], ac3[b][c], 0, 0, 0);
      if (j == 0) WAITV(17);
      if (j == 0 && ssm && lane == 0) __hip_atomic_store((GAS unsigned*)ctl + C_FA + (tn >> 1) * 128 + (tn & 1) * 36 + 4 * tm + wid, E, RLX);
      if (j + DEPTH < 18) { ldH(s, j + DEPTH); ldW(s, j + DEPTH); }
      SB0();
    }
    STAMP(14);
    WAITL0(); BAR();
    f32x4 mine[5];
#pragma unroll
    for (int b = 0; b < 4; ++b) {
      if (b == wid) {
#pragma unroll
        for (int c = 0; c < 5; ++c) mine[c] = ac3[b][c];
      } else {
        LAS char* dst = smem + (b * 3 + ((wid - b - 1) & 3)) * 5120 + v16;
#pragma unroll
        for (int c = 0; c < 5; ++c) *(LAS f32x4*)(dst + c * 1024) = ac3[b][c];
      }
    }
    WAITL0(); BAR();
    LAS char* src = smem + wid * 3 * 5120 + v16;
    const f32x4 gv = *(LAS f32x4*)(smem + L_AT + 9216 + (wid * 16 + fq * 4) * 4);
    const int t0 = t3 * 64 + wid * 16 + fq * 4;
#pragma unroll
    for (int c = 0; c < 5; ++c) {
      const f32x4 v = mine[c] + *(LAS f32x4*)(src + c * 1024) + (*(LAS f32x4*)(src + 5120 + c * 1024) + *(LAS f32x4*)(src + 10240 + c * 1024));
      const u32x2 av = *(LAS u32x2*)(smem + L_AT + (c * 16 + (c < 4 ? fr : (fr & 7))) * 128 + (wid * 16 + fq * 4) * 2);
      const unsigned p0 = pk2(gv[0] * bflo(av[0]) + (1.f - gv[0]) * v[0], gv[1] * bfhi(av[0]) + (1.f - gv[1]) * v[1]);
      const unsigned p1 = pk2(gv[2] * bflo(av[1]) + (1.f - gv[2]) * v[2], gv[3] * bfhi(av[1]) + (1.f - gv[3]) * v[3]);
      if (c < 4 || fr < 8) { u16* o = out + (size_t)t0 * 576 + dn * 72 + c * 16 + fr;
        o[0] = (u16)p0; o[576] = (u16)(p0 >> 16); o[1152] = (u16)p1; o[1728] = (u16)(p1 >> 16); }
    }
  }
  STAMP(15);
}

extern "C" void kernel_launch(void* const* d_in, const int* in_sizes, int n_in, void* d_out, int out_size, void* d_ws, size_t ws_size, hipStream_t stream) {
  (void)in_sizes; (void)n_in; (void)out_size; (void)ws_size;
  (void)hipFuncSetAttribute((const void*)gmix_fused, hipFuncAttributeMaxDynamicSharedMemorySize, LDS_BYTES);
  (void)hipMemsetAsync((char*)d_ws + OFF_ST, 0, WS_END - OFF_ST, stream);
  gmix_fused<<<256, 384, LDS_BYTES, stream>>>((const u16*)d_in[0], (const u16*)d_in[1], (const u16*)d_in[2], (const u16*)d_in[3], (const u16*)d_in[4], (const u16*)d_in[5],
      (const u16*)d_in[6], (const u16*)d_in[8], (const u16*)d_in[9], (const u16*)d_in[10], (const u16*)d_in[11], (const u16*)d_in[12], (u16*)d_out, (char*)d_ws
  );
}
```

```cpp
#include <hip/hip_runtime.h>
#include <hip/hip_bf16.h>

#define LAS __attribute__((address_space(3)))
#define GAS __attribute__((address_space(1)))
typedef unsigned short u16;
typedef unsigned u32x4 __attribute__((ext_vector_type(4)));
typedef unsigned u32x2 __attribute__((ext_vector_type(2)));
typedef short bf16x8 __attribute__((ext_vector_type(8)));
typedef short s16x4 __attribute__((ext_vector_type(4)));
typedef _Float16 f16x8 __attribute__((ext_vector_type(8)));
typedef _Float16 f16x2 __attribute__((ext_vector_type(2)));
typedef __bf16 bfx2 __attribute__((ext_vector_type(2)));
typedef float f32x4 __attribute__((ext_vector_type(4)));
typedef float f32x2 __attribute__((ext_vector_type(2)));

constexpr int SZ_F = 9437184;
constexpr int OFF_Z2 = 0, OFF_H = SZ_F, OFF_WF = 2 * SZ_F, OFF_XT = OFF_WF + 2654208, OFF_AT = OFF_XT + 2359296,
              OFF_GATE = OFF_AT + 2359296, OFF_ST = OFF_GATE + 8192, OFF_CTL = OFF_ST + 524288, WS_END = OFF_CTL + 32768;
constexpr int C_F2 = 1024, C_F3 = 2048, C_FW = 3072, C_FA = 3584;
constexpr int ZST = 40960, GST = 34816, L_PARK = 104448, L_KG = 122880, L_PS = 122880, L_RN = 124928, L_RT = 125952, L_KC = 157696, L_AT = 146688, L_US = 73728,
              L_PROG = 163072, L_BIAS = 163328, L_DUMP = 163584, LDS_BYTES = 163840;
constexpr int CV_W = 25856, CV_U = 18176, L_WPF = 103424, JL = 2;
#define HPRIO 2
#define LPRIO 3
#define PROGJ 11
#define HS 2
#define FS 7
#define TMODE 1
#define WSPT 1
constexpr unsigned SPIN_CAP = 1u << 22;

#define AUXH 16
#define AUXW 16
#define BAR() do { asm volatile("" ::: "memory"); __builtin_amdgcn_s_barrier(); asm volatile("" ::: "memory"); } while (0)
#define WAITV(n) asm volatile("s_waitcnt vmcnt(%0)" ::"i"(n) : "memory")
#define WAITL0() do { __builtin_amdgcn_s_waitcnt(0xC07F); asm volatile("" ::: "memory"); } while (0)
#define SB0() __builtin_amdgcn_sched_barrier(0)
#define RLX __ATOMIC_RELAXED, __HIP_MEMORY_SCOPE_AGENT
#define STAMP(k)
typedef __amdgpu_buffer_rsrc_t rsrc_t;
__device__ __forceinline__ rsrc_t mkrs(const void* p, int bytes) { return __builtin_amdgcn_make_buffer_rsrc((void*)p, 0, bytes, 0x00020000); }
__device__ __forceinline__ void dma16(rsrc_t r, LAS char* dst, int voff, int soff) { __builtin_amdgcn_raw_ptr_buffer_load_lds(r, (LAS void*)dst, 16, voff, soff, 0, 0); }
__device__ __forceinline__ void dma16c(rsrc_t r, LAS char* dst, int voff, int soff) { __builtin_amdgcn_raw_ptr_buffer_load_lds(r, (LAS void*)dst, 16, voff, soff, 0, 16); }
__device__ __forceinline__ unsigned pk2(float lo, float hi) { f32x2 v = {lo, hi}; return __builtin_bit_cast(unsigned, __builtin_convertvector(v, bfx2)); }
__device__ __forceinline__ float bflo(unsigned v) { return __uint_as_float(v << 16); }
__device__ __forceinline__ float bfhi(unsigned v) { return __uint_as_float(v & 0xffff0000u); }
__device__ __forceinline__ float bf2f(u16 v) { return __uint_as_float(((unsigned)v) << 16); }
__device__ __forceinline__ float rbf(float f) { return bflo(pk2(f, f)); }
__device__ __forceinline__ f32x2 gelu2(f32x2 v) {
  const f32x2 p = (v * v) * (-0.10294324f) + (-2.3022082f);
  const f32x2 a = v * p;
  f32x2 d; d.x = __builtin_amdgcn_exp2f(a.x); d.y = __builtin_amdgcn_exp2f(a.y);
  d = d + 1.0f;
  f32x2 r; r.x = __builtin_amdgcn_rcpf(d.x); r.y = __builtin_amdgcn_rcpf(d.y);
  return v * r;
}
template <int CTRL> __device__ __forceinline__ float dppadd(float x) { return x + __builtin_amdgcn_update_dpp(0.f, x, CTRL, 0xf, 0xf, true); }
template <int CTRL> __device__ __forceinline__ float dppmax(float x) { return fmaxf(x, __builtin_amdgcn_update_dpp(0.f, x, CTRL, 0xf, 0xf, true)); }
__device__ __forceinline__ float rowsum16(float x) { x = dppadd<0x128>(x); x = dppadd<0x124>(x); x = dppadd<0x122>(x); x = dppadd<0x121>(x); return x; }
template <int SLP = 1>
__device__ __forceinline__ void wait_flags(const unsigned* f, bool act, unsigned E) {
  for (unsigned sp = 0; sp < SPIN_CAP; ++sp) {
    const unsigned v = __hip_atomic_load((GAS unsigned*)f, RLX);
    if (__all(!act || v == E)) break;
    __builtin_amdgcn_s_sleep(SLP);
  }
  asm volatile("" ::: "memory");
}
template <int AUX> __device__ __forceinline__ void touch_(LAS char* smem, const void* p) { __builtin_amdgcn_global_load_lds((const GAS unsigned*)p, (LAS unsigned*)(smem + L_DUMP), 4, 0, AUX); }
__device__ __forceinline__ void touch_asm(const void* p) { unsigned keep;
  asm volatile("s_mov_b32 %0, m0\n\ts_mov_b32 m0, %2\n\ts_nop 0\n\tglobal_load_lds_dword %1, off\n\ts_mov_b32 m0, %0" : "=&s"(keep) : "v"(p), "s"((unsigned)L_DUMP) : "memory"); }
#define touch(p) touch_<0>(smem, p)
#define touchc(p) touch_<16>(smem, p)

__device__ __forceinline__ float fmul_(float a, float b) { float r; asm("v_mul_f32 %0, %1, %2" : "=v"(r) : "v"(a), "v"(b)); return r; }
__device__ __forceinline__ float ffma_(float a, float b, float c) { float r; asm("v_fma_f32 %0, %1, %2, %3" : "=v"(r) : "v"(a), "v"(b), "v"(c)); return r; }
__device__ __forceinline__ float fnma_(float a, float b, float c) { float r; asm("v_fma_f32 %0, -%1, %2, %3" : "=v"(r) : "v"(a), "v"(b), "v"(c)); return r; }
__device__ __forceinline__ f32x2 cmul(f32x2 a, f32x2 b) { return (f32x2){fnma_(a.y, b.y, fmul_(a.x, b.x)), ffma_(a.y, b.x, fmul_(a.x, b.y))}; }
__device__ __forceinline__ f32x2 cmlc(f32x2 a, f32x2 b) { return (f32x2){ffma_(a.y, b.y, fmul_(a.x, b.x)), fnma_(a.x, b.y, fmul_(a.y, b.x))}; }
__device__ __forceinline__ float ssm_par(const u32x4 a, unsigned ldtv, int h, f32x4& p) {
  const float dt = rbf(__expf(__uint_as_float(ldtv << 16)));
  float mx = fmaxf(fmaxf(fabsf(bflo(a[2])), fabsf(bfhi(a[2]))), fmaxf(fabsf(bflo(a[3])), fabsf(bfhi(a[3]))));
  mx = dppmax<0x128>(mx); mx = dppmax<0x124>(mx); mx = dppmax<0x122>(mx); mx = dppmax<0x121>(mx);
  const unsigned mb = __float_as_uint(mx);
  const float mw = fmaxf(fmaxf(__uint_as_float(__builtin_amdgcn_readlane(mb, 0)), __uint_as_float(__builtin_amdgcn_readlane(mb, 16))),
                         fmaxf(__uint_as_float(__builtin_amdgcn_readlane(mb, 32)), __uint_as_float(__builtin_amdgcn_readlane(mb, 48))));
  const int ex = (mw > 0.f && mw < 3.0e38f) ? __builtin_amdgcn_frexp_expf(mw) : 0;
  const int sh = h ? 0 : 16;
  const float la = bfhi(a[0] << sh), ai = bfhi(a[1] << sh), cr = bfhi(a[2] << sh), ci = bfhi(a[3] << sh);
  p = (f32x4){-rbf(__expf(la)) * dt * 1.44269504f, ai * dt * 0.15915494f, __builtin_amdgcn_ldexpf(cr, -ex), __builtin_amdgcn_ldexpf(ci, -ex)};
  return __builtin_amdgcn_ldexpf(2.f * dt, ex);
}
__device__ __forceinline__ f16x2 tohf(f32x2 v) { return __builtin_convertvector(v, f16x2); }
__device__ __forceinline__ f16x2 hmul(f16x2 a, f16x2 b) { return __builtin_elementwise_fma((f16x2){-b.y, b.x}, (f16x2){a.y, a.y}, b * (f16x2){a.x, a.x}); }
__device__ __forceinline__ f16x2 hmlc(f16x2 a, f16x2 b) { return __builtin_elementwise_fma((f16x2){a.y, -a.x}, (f16x2){b.y, b.y}, a * (f16x2){b.x, b.x}); }
template <int TS>
__device__ __forceinline__ void ssm_kgen(const f32x4 p, LAS char* kb, int lane, f32x4 (&kd)[3]) {
  int tb = 0;
  auto put = [&](int row, f16x2 v) { *(LAS f16x2*)(kb + tb + row * 272 + lane * 4) = v; };
  LAS char* rp = kb + (lane & 15) * 272 + (lane >> 4) * 16;
  f32x2 w[12];
#pragma unroll
  for (int k = 0; k < 12; ++k) {
    if (k % 3 == 0) { const float sc = (float)(1 << k), e = __builtin_amdgcn_exp2f(p[0] * sc), ph = __builtin_amdgcn_fractf(p[1] * sc);
      w[k] = (f32x2){e * __builtin_amdgcn_cosf(ph), e * __builtin_amdgcn_sinf(ph)}; }
    else { const f32x2 v = w[k - 1]; w[k] = (f32x2){fnma_(v.y, v.y, fmul_(v.x, v.x)), fmul_(v.x + v.x, v.y)}; } }
  f32x2 y[8];
  y[0] = (f32x2){p[2], -p[3]}; y[1] = cmlc(y[0], w[3]); y[2] = cmlc(y[0], w[4]); y[3] = cmlc(y[1], w[4]);
#pragma unroll
  for (int b = 0; b < 4; ++b) y[4 + b] = cmlc(y[b], w[5]);
  const f16x2 h6 = tohf(w[6]), h7 = tohf(w[7]), h10 = tohf(w[10]);
#pragma unroll
  for (int b = 0; b < 8; ++b) { const f16x2 hy = tohf(y[b]); put(b, hy); put(8 + b, hmlc(hy, h6)); }
  f16x8 fb[4];
  auto rdb = [&]() {
#pragma unroll
    for (int k = 0; k < 4; ++k) fb[k] = *(LAS f16x8*)(rp + k * 64); };
  auto tile_ = [&](int t) { kd[t] = (f32x4){0, 0, 0, 0};
#pragma unroll
    for (int k = 0; k < 4; ++k) kd[t] = __builtin_amdgcn_mfma_f32_16x16x32_f16(*(LAS f16x8*)(rp + (t + 1) * TS + k * 64), fb[k], kd[t], 0, 0, 0); };
  auto tile = [&](int t) { if (TS == 0) tile_(t); tb += TS; };
  if (TS == 0) rdb();
  tb += TS;
  f16x2 g[8];
  { const f32x2 g3 = cmul(w[0], w[1]);
    g[0] = (f16x2){(_Float16)1.f, (_Float16)0.f}; g[1] = tohf(w[0]); g[2] = tohf(w[1]); g[3] = tohf(g3); g[4] = tohf(w[2]);
    g[5] = tohf(cmul(w[2], w[0])); g[6] = tohf(cmul(w[2], w[1])); g[7] = tohf(cmul(w[2], g3)); }
#pragma unroll
  for (int c = 0; c < 8; ++c) { put(c, g[c]); put(8 + c, c ? hmul(g[c], h7) : h7); }
  tile(0);
  f16x2 f[8];
  { const f32x2 f3 = cmul(w[8], w[7]);
    f[1] = h7; f[2] = tohf(w[8]); f[3] = tohf(f3); f[4] = tohf(w[9]); f[5] = tohf(cmul(w[9], w[7])); f[6] = tohf(cmul(w[9], w[8])); f[7] = tohf(cmul(w[9], f3)); }
#pragma unroll
  for (int a = 0; a < 2; ++a)
#pragma unroll
    for (int c = 0; c < 4; ++c) put(4 * a + c, c ? hmul(f[2 + a], g[2 * c]) : f[2 + a]);
#pragma unroll
  for (int a = 0; a < 4; ++a) { put(8 + 2 * a, f[4 + a]); put(9 + 2 * a, hmul(f[4 + a], g[4])); }
  tile(1);
#pragma unroll
  for (int a = 0; a < 8; ++a) put(a, a ? hmul(h10, f[a]) : h10);
  put(8, tohf(w[11]));
  tile(2);
  if (TS) { SB0(); rdb(); tile_(0); tile_(1); tile_(2); }
}

struct ZF { bf16x8 x[4]; s16x4 w[5][2]; };

__global__ __launch_bounds__(512, 2) void gmix_fused(const u16* __restrict__ x, const u16* __restrict__ lar, const u16* __restrict__ aim, const u16* __restrict__ cre,
    const u16* __restrict__ cim, const u16* __restrict__ ldt, const u16* __restrict__ dsk, const u16* __restrict__ win, const u16* __restrict__ wsp,
    const u16* __restrict__ bsp, const u16* __restrict__ wout, const u16* __restrict__ gw, u16* __restrict__ out, char* ws
) {
  extern __shared__ __attribute__((aligned(16))) char smem_[];
  LAS char* smem = (LAS char*)smem_;
  const int wid = __builtin_amdgcn_readfirstlane(threadIdx.x >> 6);
  int lane = threadIdx.x & 63;
  const int bx = blockIdx.x & 7, bl = blockIdx.x >> 3;
  const int tm = (bx >> 2) * 8 + (bl & 7), tn = (bx & 3) * 4 + (bl >> 3);
  const int pp = tn >> 1, ee = tn & 1;
  const int t3 = bx * 4 + (bl & 3), dn = bl >> 2;
  unsigned* ctl = (unsigned*)(ws + OFF_CTL);
  const unsigned E = 1u;
  if (wid != 7) STAMP(0);
  int fr = lane & 15, fq = lane >> 4, v16 = lane * 16;
  const bool ssm = tm < 9;
  const int wcol = (288 * tn) & ~127;
  const int uP = (tm - 9) * 16 + tn;
  u32x4 sj[6][2]; unsigned pdt2 = 0;
  const bool isL = wid == 4 || wid == 5, gjob = !ssm && uP < 72 && !isL;
  const int grow = (uP * 6 + (wid < 4 ? wid : wid - 2)) * 5;
  auto jobload = [&](int jb) {
    if (ssm) { const int d = 36 * tn + 4 * tm + (jb & 3), o = d * 64 + lane;
      sj[0][0] = (u32x4){((const unsigned*)lar)[o], ((const unsigned*)aim)[o], ((const unsigned*)cre)[o], ((const unsigned*)cim)[o]};
      pdt2 = (((const unsigned*)ldt)[d >> 1] >> (16 * (d & 1))) & 0xffffu;
    } else if (gjob) {
#pragma unroll
      for (int k = 0; k < 2; ++k) { const int c = k ? 64 + (lane & 7) : lane; sj[5][k] = *(const u32x4*)(gw + c * 8);
#pragma unroll
        for (int r = 0; r < 5; ++r) sj[r][k] = *(const u32x4*)(x + (size_t)(grow + r < 2048 ? grow + r : 2047) * 576 + c * 8); }
    } };
  if (!isL) { jobload(wid);
#pragma unroll
    for (int k = 0; k < 6; ++k) asm volatile("" : "+v"(sj[k][0]), "+v"(sj[k][1])); }
  else if (TMODE != 2) {
    const int d = ssm ? 36 * tn + 4 * tm + (wid & 3) : 0, o = d * 64 + lane;
    unsigned a0, a1, a2, a3;
    asm volatile("global_load_dword %0, %4, off\n\tglobal_load_dword %1, %5, off\n\tglobal_load_dword %2, %6, off\n\tglobal_load_dword %3, %7, off"
                 : "=&v"(a0), "=&v"(a1), "=&v"(a2), "=&v"(a3)
                 : "v"((const unsigned*)lar + o), "v"((const unsigned*)aim + o), "v"((const unsigned*)cre + o), "v"((const unsigned*)cim + o) : "memory");
    const bool isx = lane < 32 || lane >= 56; const int l3 = (lane - 32) / 3;
    const char* tsh = isx ? (const char*)x + (size_t)(tm * 128 + (bl >> 3) * 32 + (lane & 31)) * 1152 + (wid & 1) * 128
                          : (const char*)win + (size_t)(64 * (wid & 1) + 8 * (bl & 7) + l3) * 9216 + wcol + 4608 + (lane - 32 - 3 * l3) * 128;
    const int tst = isx ? 256 : 128 * 9216;
#pragma unroll
    for (int k = 0; k < 5; ++k) touch_asm(tsh + (size_t)(k == 4 && (wid & 1) ? 3 : k) * tst);
    asm volatile("s_waitcnt vmcnt(5)" : "+v"(a0), "+v"(a1), "+v"(a2), "+v"(a3) :: "memory");
    sj[0][0] = (u32x4){a0, a1, a2, a3};
    pdt2 = (((const unsigned*)ldt)[d >> 1] >> (16 * (d & 1))) & 0xffffu;
  }
  const int lw = wid - 4;
  const rsrc_t rX = mkrs(x + (size_t)tm * 128 * 576, 128 * 1152);
  const rsrc_t rWi = mkrs(win, 576 * 9216);
  int vxP[2], vxN[2], vw[6];
  auto zaddr = [&](int ln) {
#pragma unroll
    for (int h = 0; h < 2; ++h) {
      const int swz = ((ln & 7) ^ ((4 * h + (ln >> 4)) & 7)) << 4;
      const int i = 8 * h + (ln >> 3);
      vxP[h] = (8 * (i >> 2) + (i & 3)) * 1152 + swz;
      vxN[h] = i * 1152 + swz;
    }
#pragma unroll
    for (int q = 0; q < 6; ++q) { const int o = q * 1024 + ln * 16, row = o / 384, pos = (o - row * 384) >> 4; vw[q] = row * 9216 + ((pos ^ ((row & 2) | ((row & 8) >> 1))) << 4); }
  };
  auto relane = [&]() { int z = 0; asm volatile("" : "+v"(z));
    lane = __builtin_amdgcn_mbcnt_hi(~0u, __builtin_amdgcn_mbcnt_lo(~0u, z)); fr = lane & 15; fq = lane >> 4; v16 = lane * 16; };
  zaddr(lane);
  auto issueZ = [&](int g, int slot) {
    const bool z2 = g < 9; const int kt = z2 ? g : g - 9;
    LAS char* dst = smem + slot;
    const int sw = kt * 64 * 9216 + wcol + (z2 ? 4608 : 0);
    if (lw == 0) {
      const int v0 = z2 ? vxP[0] : vxN[0], v1 = z2 ? vxP[1] : vxN[1], odd = (z2 ? 4 : 16) * 1152;
#pragma unroll 1
      for (int t2 = 0; t2 < 4; ++t2) { const int so = t2 * 32 * 1152 + kt * 128; LAS char* d = dst + t2 * 4096;
        dma16(rX, d, v0, so); dma16(rX, d + 1024, v1, so); dma16(rX, d + 2048, v0, so + odd); dma16(rX, d + 3072, v1, so + odd); }
#pragma unroll
      for (int q = 0; q < 4; ++q) dma16(rWi, dst + 16384 + q * 1024, vw[q], sw);
    } else {
      dma16(rWi, dst + 16384 + 4096, vw[4], sw); dma16(rWi, dst + 16384 + 5120, vw[5], sw);
#pragma unroll 1
      for (int q6 = 1; q6 < 4; ++q6)
#pragma unroll
        for (int q = 0; q < 6; ++q) dma16(rWi, dst + 16384 + (q6 * 6 + q) * 1024, vw[q], sw + q6 * 16 * 9216);
    }
  };
  if (isL && LPRIO) __builtin_amdgcn_s_setprio(LPRIO);
  if (TMODE == 2 && isL) {
#pragma unroll 1
    for (int g = 0; g < 2; ++g) issueZ(g, g * ZST);
  }
  f32x4 kd[3]; float ksc = 0.f;
  if (TMODE != 2 || !isL) {
#pragma unroll 1
    for (int jb = wid;; jb -= 2) {
      if (ssm) {
        f32x4 p; ksc = ssm_par(sj[0][0], pdt2, jb >> 2, p);
        if (TMODE == 2) ssm_kgen<0>(p, smem + L_KG + jb * 4352, lane, kd);
        else ssm_kgen<4352>(p, smem + (jb < 7 ? jb * 17408 : L_KG + 12288), lane, kd);
        if (jb >= 4) {
#pragma unroll
          for (int t = 0; t < 3; ++t) *(LAS f32x4*)(smem + L_KG + (TMODE == 2 ? jb * 4352 : (jb - 4) * 3072) + t * 1024 + v16) = kd[t];
        }
      } else if (gjob) {
        float sv4 = 0.f;
#pragma unroll
        for (int r = 0; r < 5; ++r) { float s0 = 0.f, s1 = 0.f;
#pragma unroll
          for (int j = 0; j < 4; ++j) { s0 += bflo(sj[r][0][j]) * bflo(sj[5][0][j]) + bfhi(sj[r][0][j]) * bfhi(sj[5][0][j]); s1 += bflo(sj[r][1][j]) * bflo(sj[5][1][j]) + bfhi(sj[r][1][j]) * bfhi(sj[5][1][j]); }
          s0 += lane < 8 ? s1 : 0.f;
          s0 = rowsum16(s0); s0 += __shfl_xor(s0, 16); s0 += __shfl_xor(s0, 32);
          sv4 = lane == r ? s0 : sv4; }
        if (lane < 5 && grow + lane < 2048) __hip_atomic_store((GAS unsigned*)(ws + OFF_GATE) + grow + lane, __float_as_uint(__builtin_amdgcn_rcpf(1.f + __builtin_amdgcn_exp2f(-1.44269504f * sv4))), RLX);
      }
      if (TMODE != 2 || jb < 6 || !ssm) break;
      jobload(jb - 2);
    }
  }
  if (TMODE != 2) { WAITL0(); BAR(); }

  if (wid >= 6) {
    relane();
    const int hw = wid - 6;
    const bool retile = !ssm && uP < 72;
    if (hw == 0) { __builtin_amdgcn_global_load_lds((const GAS unsigned*)(bsp + tm * 128 + 2 * lane), (LAS unsigned*)(smem + L_BIAS), 4, 0, 0);
      if (lane == 0) *(LAS unsigned*)(smem + L_PROG) = 0u; }
    WAITL0();
#pragma unroll 1
    for (int g = -1; g < 9; ++g) { if (g == 4) WAITV(0); BAR(); }
    const int p9 = uP / 9, r9 = uP - p9 * 9; const bool nat = r9 == 8;
    LAS char* rs = smem + L_RT + hw * 18432;
    if (retile) {
      const rsrc_t rO = mkrs(wout, 2304 * 1152);
#pragma unroll 2
      for (int k = 0; k < 18; ++k) { const int c = k * 64 + lane, row = c / 36, ch = c - row * 36;
        const int f = nat ? 144 * (2 * p9 + (row >> 4)) + 128 + (row & 15) : 144 * (2 * p9 + (r9 >> 2)) + 32 * (r9 & 3) + row;
        dma16(rO, rs + k * 1024, f * 1152 + hw * 576 + ch * 16, 0); }
    }
    {
      const char* wv = (const char*)win + (size_t)(192 + (bl & 7) * 48 + hw * 24 + (lane >> 2)) * 9216 + wcol + ((lane & 3) < 3 ? (lane & 3) : 2) * 128;
      const char* sv = (const char*)wsp + (size_t)(blockIdx.x * 2 + hw) * 16384 + lane * 128;
      touch(wv); if (lane < 32) touch(wv + 16 * 9216);
      if (WSPT) { touch(sv); touch(sv + 8192); } }
    if (retile) {
      BAR(); BAR(); BAR();
      WAITV(0);
      const int q4 = fr >> 2, p4 = lane & 3;
      const rsrc_t rF = mkrs(ws + OFF_WF, 2654208);
#pragma unroll 1
      for (int dl = 0; dl < 4; ++dl) {
#pragma unroll
        for (int c = 0; c < 5; ++c) { s16x4 h[2];
#pragma unroll
          for (int t = 0; t < 2; ++t) { const int row = nat ? 8 * fq + 4 * t + q4 : 16 * t + 4 * fq + q4; int col = 72 * dl + 16 * c + 4 * p4; col = col > 284 ? 284 : col;
            h[t] = __builtin_amdgcn_ds_read_tr16_b64_v4i16((LAS s16x4*)(rs + row * 576 + col * 2)); }
          const u32x4 v = __builtin_bit_cast(u32x4, __builtin_shufflevector(h[0], h[1], 0, 1, 2, 3, 4, 5, 6, 7));
          const int so = ((4 * hw + dl) * 72 + uP) * 4608 + c * 1024;
          if (c < 4) __builtin_amdgcn_raw_buffer_store_b128(v, rF, v16, so, 16);
          else if (fr < 8) __builtin_amdgcn_raw_buffer_store_b128(v, rF, (fq * 8 + fr) * 16, so, 16);
        }
        WAITL0(); BAR();
      }
      BAR(); BAR(); BAR(); BAR();
      WAITV(0);
      if (lane == 0) __hip_atomic_store((GAS unsigned*)ctl + C_FW + uP * 2 + hw, E, RLX);
    }
    STAMP(4);
    return;
  }

  if (wid >= 4) {
    relane(); zaddr(lane);
    {
      if (TMODE != 2) { WAITV(0); STAMP(3);
#pragma unroll 1
        for (int g = 0; g < 2; ++g) issueZ(g, g * ZST); }
      WAITV(20);
      STAMP(6);
      BAR();
      issueZ(2, 2 * ZST);
      int zs = 0;
#pragma unroll 1
      for (int g = 0; g < 15; ++g) { WAITV(20); BAR(); issueZ(g + 3, zs); zs = (zs == 2 * ZST) ? 0 : zs + ZST; }
    }
    const rsrc_t rW = mkrs(wsp + (size_t)tm * 128 * 2048, 128 * 4096);
    const rsrc_t rZ = mkrs(ws + OFF_Z2, SZ_F);
    const int vW0 = (lane >> 3) * 4096 + (((lane & 7) ^ (lane >> 4)) << 4), vW1 = (lane >> 3) * 4096 + (((lane & 7) ^ (4 + (lane >> 4))) << 4);
    auto issueW = [&](int kt, int slot) {
      LAS char* dst = smem + slot * GST;
#pragma unroll
      for (int p = 0; p < 16; ++p) dma16(rW, dst + p * 1024, (p & 1) ? vW1 : vW0, p * 8 * 4096 + kt * 128);
    };
    auto issueZ2 = [&](int kt, int slot) {
      LAS char* dst = smem + slot * GST + 16384; const int so = (kt * 144 + 9 * tn) * 2048;
#pragma unroll
      for (int p = 0; p < 18; ++p) dma16c(rZ, dst + p * 1024, v16, so + p * 1024);
    };
    if (lw == 0) {
      WAITV(20); BAR(); issueW(0, 0);
      WAITV(16); BAR(); issueW(1, 1);
      BAR(); issueW(2, 2);
      WAITV(32); BAR();
      int slot = 0;
#pragma unroll 1
      for (int i = 0; i < 29; ++i) { WAITV(16); BAR(); issueW(i + 3, slot); slot = (slot == 2) ? 0 : slot + 1; }
      WAITV(16); BAR();
      if (ssm) {
        const rsrc_t rU = mkrs(ws + OFF_XT + (size_t)tn * 64 * 2304, 64 * 2304);
#pragma unroll 4
        for (int p = 0; p < 16; ++p) dma16c(rU, smem + L_US + p * 1024, (lane >> 2) * 2304 + (lane & 3) * 16, (p & 3) * 16 * 2304 + (4 * tm + (p >> 2)) * 64);
        WAITV(16);
      } else WAITV(0);
      BAR();
      BAR();
    } else {
      WAITV(20); BAR();
      WAITV(0); BAR();
      BAR();
      wait_flags(ctl + C_F2 + tn * 64 + lane, true, E);
      STAMP(1);
#pragma unroll 1
      for (int i = 0; i < 3; ++i) issueZ2(i, i);
      WAITV(36); BAR();
      int slot = 0;
#pragma unroll 1
      for (int i = 0; i < 29; ++i) { WAITV(18); BAR(); issueZ2(i + 3, slot); slot = (slot == 2) ? 0 : slot + 1; }
      WAITV(18); BAR();
      WAITV(0); BAR();
      BAR();
    }
    BAR();
    WAITV(0); BAR();
    BAR();
    if (lw == 0) {
      for (int k = 0; k < 3; ++k) wait_flags(ctl + C_FW + k * 64 + lane, k * 64 + lane < 144, E);
      const rsrc_t rF = mkrs(ws + OFF_WF + (size_t)dn * 72 * 4608, 72 * 4608);
#pragma unroll 4
      for (int p = 0; p < JL * 18; ++p) dma16c(rF, smem + L_WPF + p * 1024, v16, p * 1024);
      { const char* fv = ws + OFF_WF + (size_t)dn * 72 * 4608 + JL * 4 * 4608 + (size_t)(bl & 3) * 73728 + lane * 128;
#pragma unroll
        for (int k = 0; k < 9; ++k) touchc(fv + k * 8192); }
      WAITV(9);
    } else {
      wait_flags(ctl + C_F3 + (t3 >> 1) * 64 + lane, true, E);
      STAMP(2);
      { const char* hv = ws + OFF_H + (size_t)t3 * 4 * 73728 + (size_t)dn * 36864 + lane * 128;
#pragma unroll
        for (int k = 0; k < 4; ++k) touchc(hv + k * 8192);
        if (lane < 32) touchc(hv + 4 * 8192); }
    }
    BAR();
    if (lw == 0) {
      for (unsigned sp = 0; sp < (SPIN_CAP << 6) && *(volatile LAS unsigned*)(smem + L_PROG) == 0u; ++sp) __builtin_amdgcn_s_sleep(4);
      wait_flags<8>(ctl + C_FA + dn * 64 + lane, lane < 18, E);
      const rsrc_t rA = mkrs(ws + OFF_AT + (size_t)dn * 72 * 4096, 72 * 4096);
#pragma unroll
      for (int p = 0; p < 9; ++p) dma16c(rA, smem + L_AT + p * 1024, (lane >> 3) * 4096 + (lane & 7) * 16, p * 8 * 4096 + t3 * 128);
      const rsrc_t rG = mkrs(ws + OFF_GATE + t3 * 256, 256);
      dma16c(rG, smem + L_AT + 9216, v16, 0);
    } else if (ssm) {
      const rsrc_t rA = mkrs(ws + OFF_AT + (size_t)(36 * tn + 4 * tm) * 4096, 4 * 4096);
#pragma unroll 4
      for (int p = 0; p < 16; ++p) __builtin_amdgcn_raw_buffer_store_b128(*(LAS u32x4*)(smem + (p >> 2) * CV_W + (p & 3) * 1024 + v16), rA, v16, p * 1024, 16);
      WAITV(0);
      if (lane == 0) __hip_atomic_store((GAS unsigned*)ctl + C_FA + (tn >> 1) * 64 + (tn & 1) * 9 + tm, E, RLX);
    }
    WAITV(0);
    BAR();
    BAR();
    return;
  }

  relane();
  const int dch = 36 * tn + 4 * tm + wid;
  const int dsf = ssm ? dch : 0;
  const float dskf = __uint_as_float((((const unsigned*)dsk)[dsf >> 1] >> (16 * (dsf & 1))) << 16);
  const int rh = wid >> 1, ch = wid & 1;
  int xo[2][2], wo[5];
#pragma unroll
  for (int kk = 0; kk < 2; ++kk)
#pragma unroll
    for (int h = 0; h < 2; ++h) xo[kk][h] = (64 * rh + 32 * (h ^ ch) + fr) * 128 + (((4 * kk + fq) ^ ((fr >> 1) & 7)) << 4);
  { const int q4 = fr >> 2, p4 = lane & 3, cb = ((288 * tn) & 127) >> 4, sz = (q4 & 2) | ((fq & 1) << 2);
#pragma unroll
    for (int b = 0; b < 5; ++b) wo[b] = 16384 + (8 * fq + q4) * 384 + (((cb + 2 * (b & 3) + (p4 >> 1)) ^ sz) << 4) + 8 * (p4 & 1) + (b < 4 ? 128 * ch : 256); }
  auto rdz = [&](ZF& F, int sl, int kk) {
    LAS char* s = smem + sl;
#pragma unroll
    for (int i = 0; i < 4; ++i) F.x[i] = *(LAS bf16x8*)(s + xo[kk][i >> 1] + (i & 1) * 2048);
#pragma unroll
    for (int b = 0; b < 5; ++b)
#pragma unroll
      for (int t = 0; t < 2; ++t) F.w[b][t] = __builtin_amdgcn_ds_read_tr16_b64_v4i16((LAS s16x4*)(s + wo[b] + (32 * kk + 4 * t) * 384));
  };
  f32x4 za[4][4], z8[2];
  auto zzero = [&]() {
#pragma unroll
    for (int i = 0; i < 4; ++i)
#pragma unroll
      for (int b = 0; b < 4; ++b) za[i][b] = (f32x4){0, 0, 0, 0};
    z8[0] = (f32x4){0, 0, 0, 0}; z8[1] = (f32x4){0, 0, 0, 0}; };
  auto mmz = [&](const ZF& F, bool z2) {
#pragma unroll
    for (int b = 0; b < 5; ++b) { const bf16x8 wv = __builtin_shufflevector(F.w[b][0], F.w[b][1], 0, 1, 2, 3, 4, 5, 6, 7);
#pragma unroll
      for (int i = 0; i < (b < 4 ? 4 : 2); ++i) { f32x4& a = b < 4 ? za[i][b & 3] : z8[i & 1];
        a = z2 ? __builtin_amdgcn_mfma_f32_16x16x32_bf16(F.x[i], wv, a, 0, 0, 0) : __builtin_amdgcn_mfma_f32_16x16x32_bf16(wv, F.x[i], a, 0, 0, 0); } }
#pragma unroll
    for (int i = 0; i < 14; ++i) { __builtin_amdgcn_sched_group_barrier(0x8, 1, 0); __builtin_amdgcn_sched_group_barrier(0x100, 1, 0); }
    __builtin_amdgcn_sched_group_barrier(0x8, 4, 0); SB0();
  };
  ZF F0, F1;
  int sl = 0;
  auto zh1 = [&](bool z2) { rdz(F1, sl, 1); mmz(F0, z2); };
  auto zbar = [&]() { WAITL0(); BAR(); sl = (sl == 2 * ZST) ? 0 : sl + ZST; };
  auto zh2 = [&](bool z2) { rdz(F0, sl, 0); mmz(F1, z2); };
  zzero();
  const rsrc_t rXT = mkrs(ws + OFF_XT + (size_t)(tn * 64 + tm * 4 + wid) * 2304, 2304);
  STAMP(1);
  BAR();
  STAMP(2);
  rdz(F0, 0, 0);
  if (ssm) {
    LAS char* kp = smem + L_KG + (TMODE == 2 ? (4 + wid) * 4352 : wid * 3072) + v16;
    LAS u16* kc = (LAS u16*)(smem + L_KC + wid * 1344);
#pragma unroll
    for (int t = 0; t < 3; ++t) kd[t] = (kd[t] + *(LAS f32x4*)(kp + t * 1024)) * ksc;
#pragma unroll
    for (int j = 0; j < 4; ++j) { const int r = fq * 4 + j;
      kc[128 * (r >> 3) + (r & 7) + 8 * fr] = (u16)pk2(kd[0][j], 0.f);
      kc[r < 8 ? 256 + 64 * (r >> 2) + (r & 3) + 4 * fr : 384 + 32 * ((r - 8) >> 1) + (r & 1) + 2 * fr] = (u16)pk2(kd[1][j], 0.f);
      if (r < 8 || (r == 8 && fr == 0)) kc[512 + 16 * r + fr] = (u16)pk2(kd[2][j], 0.f); }
  }
#pragma unroll 1
  for (int g = 0; g < 9; ++g) {
    zh1(true);
    if (g == (9 * tn) >> 4 || g == (9 * tn + 8) >> 4) {
      const int q4 = fr >> 2, p4 = lane & 3;
#pragma unroll
      for (int ct = 0; ct < 3; ++ct) {
        const int lq = (9 * tn + 4 * ct + p4) & 15; s16x4 h[2];
#pragma unroll
        for (int e = 0; e < 2; ++e) { const int rho = 32 * wid + 16 * e + 4 * fq + q4;
          h[e] = __builtin_amdgcn_ds_read_tr16_b64_v4i16((LAS s16x4*)(smem + sl + rho * 128 + (((lq >> 1) ^ ((rho >> 1) & 7)) << 4) + 8 * (lq & 1))); }
        const int myq = 4 * ct + (fr >> 2);
        if (myq < 9 && ((9 * tn + myq) >> 4) == g)
          __builtin_amdgcn_raw_buffer_store_b128(__builtin_bit_cast(u32x4, __builtin_shufflevector(h[0], h[1], 0, 1, 2, 3, 4, 5, 6, 7)), rXT, (16 * ct + fr) * 64 + fq * 16, 0, 16);
      }
    }
    zbar(); zh2(true);
  }
  STAMP(3);
  u32x4 zr[2][4], zr8;
#pragma unroll
  for (int P = 0; P < 2; ++P) {
    f32x2 S[4], Q[4];
#pragma unroll
    for (int k = 0; k < 4; ++k) { S[k] = (f32x2){0, 0}; Q[k] = (f32x2){0, 0}; }
#pragma unroll
    for (int b = 0; b < (P ? 4 : 5); ++b)
#pragma unroll
      for (int k = 0; k < 4; ++k) { const f32x4 c = b < 4 ? za[2 * P + (k >> 1)][b & 3] : z8[k >> 1]; f32x2 v = (k & 1) ? (f32x2){c[2], c[3]} : (f32x2){c[0], c[1]};
        v = gelu2(v); S[k] += v; Q[k] += v * v; if (b < 4) zr[P][b & 3][k] = pk2(v.x, v.y); else zr8[k] = pk2(v.x, v.y); }
#pragma unroll
    for (int k = 0; k < 4; ++k) { S[k].x = rowsum16(S[k].x); S[k].y = rowsum16(S[k].y); Q[k].x = rowsum16(Q[k].x); Q[k].y = rowsum16(Q[k].y); }
    if (fr == 0) {
      LAS f32x4* d = (LAS f32x4*)(smem + L_PS + ch * 1024 + (64 * rh + 32 * (P ^ ch) + 8 * fq) * 4);
      d[0] = (f32x4){S[0].x, S[0].y, S[1].x, S[1].y}; d[1] = (f32x4){S[2].x, S[2].y, S[3].x, S[3].y};
      d[32] = (f32x4){Q[0].x, Q[0].y, Q[1].x, Q[1].y}; d[33] = (f32x4){Q[2].x, Q[2].y, Q[3].x, Q[3].y};
    }
  }
#pragma unroll
  for (int b = 0; b < 4; ++b) asm volatile("" : "+v"(zr[0][b]), "+v"(zr[1][b]));
  asm volatile("" : "+v"(zr8));
  STAMP(4);
  zzero();
  zh1(false); zbar();
  {
    const int row = 32 * wid + 8 * fq + (fr & 7);
    LAS float* ps = (LAS float*)(smem + L_PS) + (fr >> 3) * 128 + row;
    GAS unsigned long long* gp = (GAS unsigned long long*)(ws + OFF_ST + ((tm * 16 + tn) * 128 + row) * 16 + (fr >> 3) * 8);
    __hip_atomic_store(gp, ((unsigned long long)E << 32) | __float_as_uint(ps[0] + ps[256]), RLX);
  }
  zh2(false);
#pragma unroll 1
  for (int g = 1; g < HS; ++g) { zh1(false); zbar(); zh2(false); }
  {
    const rsrc_t rS = mkrs(ws + OFF_ST + (size_t)tm * 16 * 2048, 16 * 2048);
    u32x4 gr[8];
    auto ldgr = [&]() {
#pragma unroll
      for (int k = 0; k < 8; ++k) gr[k] = __builtin_amdgcn_raw_buffer_load_b128(rS, ((lane >> 5) * 8 * 128 + 32 * wid + (lane & 31)) * 16, k * 2048, 16); };
    ldgr(); SB0();
    zh1(false); zbar(); zh2(false); zh1(false);
#pragma unroll
    for (int k = 0; k < 8; ++k) asm volatile("" : "+v"(gr[k]));
    float s, q;
    for (unsigned sp = 0;; ++sp) {
      bool ok = true; s = 0.f; q = 0.f;
#pragma unroll
      for (int k = 0; k < 8; ++k) { ok = ok && gr[k][1] == E && gr[k][3] == E; s += __uint_as_float(gr[k][0]); q += __uint_as_float(gr[k][2]); }
      if (__all(ok) || sp >= SPIN_CAP) break;
      __builtin_amdgcn_s_sleep(1);
      ldgr();
    }
    s += __shfl_xor(s, 32); q += __shfl_xor(q, 32);
    const float mean = s * (1.f / 2304.f), var = fmaxf(q * (1.f / 2304.f) - mean * mean, 0.f), rs = __builtin_amdgcn_rsqf(var + 1e-5f);
    if (lane < 32) *(LAS f32x2*)(smem + L_RN + (32 * wid + lane) * 8) = (f32x2){rs, -mean * rs};
    STAMP(22);
    zbar();
    const rsrc_t rZs = mkrs(ws + OFF_Z2, SZ_F);
#pragma unroll
    for (int P = 0; P < 2; ++P) {
      f32x4 rn[4];
#pragma unroll
      for (int k = 0; k < 4; ++k) rn[k] = *(LAS f32x4*)(smem + L_RN + (64 * rh + 32 * (P ^ ch) + 8 * fq) * 8 + k * 16);
      const int zo = (((2 * tm + rh) * 144 + 9 * tn) * 2 + (P ^ ch)) * 1024;
#pragma unroll
      for (int b = 0; b < (P ? 4 : 5); ++b) { const u32x4 z = b < 4 ? zr[P][b & 3] : zr8; u32x4 o;
#pragma unroll
        for (int k = 0; k < 4; ++k) o[k] = pk2(bflo(z[k]) * rn[k][0] + rn[k][1], bfhi(z[k]) * rn[k][2] + rn[k][3]);
        __builtin_amdgcn_raw_buffer_store_b128(o, rZs, v16, zo + (b < 4 ? 4 * ch + b : 8) * 2048, 16); }
    }
    SB0();
    STAMP(5);
    zh2(false);
  }
#pragma unroll 1
  for (int g = HS + 2; g < 9; ++g) {
    zh1(false);
    if (g == FS) { WAITV(0); if (lane == 0) __hip_atomic_store((GAS unsigned*)ctl + C_F2 + tn * 64 + tm * 4 + wid, E, RLX); }
    zbar(); zh2(false);
  }
  STAMP(6);
  {
    LAS char* pk = smem + L_PARK;
#pragma unroll
    for (int i = 0; i < 4; ++i) { const int m = 4 * rh + ((i + 2 * ch) & 3);
#pragma unroll
      for (int c = 0; c < 2; ++c) { u32x4 o;
#pragma unroll
        for (int k = 0; k < 4; ++k) { const f32x4 cc = za[i][2 * c + (k >> 1)]; f32x2 v = (k & 1) ? (f32x2){cc[2], cc[3]} : (f32x2){cc[0], cc[1]}; v = gelu2(v); o[k] = pk2(v.x, v.y); }
        *(LAS u32x4*)(pk + (m * 4 + 2 * ch + c) * 1024 + v16) = o; }
      if (i < 2) { const f32x2 v0 = gelu2((f32x2){z8[i][0], z8[i][1]}), v1 = gelu2((f32x2){z8[i][2], z8[i][3]});
        *(LAS u32x2*)(pk + 32768 + m * 512 + lane * 8) = (u32x2){pk2(v0.x, v0.y), pk2(v1.x, v1.y)}; }
    }
  }
  float bb[2];
  { LAS u16* bs = (LAS u16*)(smem + L_BIAS) + 32 * wid + fr; bb[0] = bf2f(bs[0]); bb[1] = bf2f(bs[16]); }
  STAMP(7);
  const int kh = wid & 1, th = wid >> 1;
  u32x4 ul[4];
  f32x4 acc[9][4];
  {
    const int zoff = 16384 + kh * 1024 + v16;
    const int woff = (th * 64 + fr) * 128 + (((kh * 4 + fq) ^ ((fr >> 1) & 7)) << 4);
    const int woA = woff + kh * 4096, woB = woff + 4096 - kh * 4096;
#pragma unroll
    for (int a = 0; a < 9; ++a)
#pragma unroll
      for (int b = 0; b < 4; ++b) { const float iv = b < 2 ? bb[b] : 0.f; acc[a][b] = (f32x4){iv, iv, iv, iv}; }
    bf16x8 Z0[9], W0[4], Z1[9], W1[4];
    auto rd = [&](bf16x8(&Z)[9], bf16x8(&W)[4], int sl) {
      LAS char* s = smem + sl;
#pragma unroll
      for (int b = 0; b < 4; ++b) W[b] = *(LAS bf16x8*)(s + (b < 2 ? woA : woB) + (b & 1) * 2048);
#pragma unroll
      for (int a = 0; a < 9; ++a) Z[a] = *(LAS bf16x8*)(s + zoff + a * 2048);
    };
    auto mm = [&](bf16x8(&Z)[9], bf16x8(&W)[4]) {
#pragma unroll
      for (int a = 0; a < 9; ++a)
#pragma unroll
        for (int b = 0; b < 4; ++b) acc[a][b] = __builtin_amdgcn_mfma_f32_16x16x32_bf16(Z[a], W[b], acc[a][b], 0, 0, 0);
    };
    auto sched = [&]() {
#pragma unroll
      for (int i = 0; i < 13; ++i) { __builtin_amdgcn_sched_group_barrier(0x8, 2, 0); __builtin_amdgcn_sched_group_barrier(0x100, 1, 0); }
      __builtin_amdgcn_sched_group_barrier(0x8, 10, 0); SB0();
    };
    WAITL0(); BAR();
    STAMP(8);
    rd(Z0, W0, 0);
    sl = 0;
#pragma unroll 1
    for (int i = 0; i < 32; i += 2) {
      sl = (sl == 2 * GST) ? 0 : sl + GST;
      WAITL0(); BAR();
      rd(Z1, W1, sl); mm(Z0, W0); sched();
      sl = (sl == 2 * GST) ? 0 : sl + GST;
      WAITL0(); BAR();
      rd(Z0, W0, sl); mm(Z1, W1); sched();
    }
    relane();
    STAMP(9);
    WAITL0(); BAR();
    LAS char* dst = smem + wid * 18432 + v16;
#pragma unroll
    for (int a = 0; a < 9; ++a)
#pragma unroll
      for (int u = 0; u < 2; ++u) *(LAS f32x4*)(dst + (a * 2 + u) * 1024) = acc[a][2 + u];
  }
  WAITL0(); BAR();
  STAMP(20);
  {
    const rsrc_t rHs = mkrs(ws + OFF_H, SZ_F);
    LAS char* src = smem + (wid ^ 1) * 18432 + v16;
    LAS char* pk = smem + L_PARK + wid * 8192 + v16;
    const u32x2 y8[2] = {*(LAS u32x2*)(smem + L_PARK + 32768 + wid * 1024 + lane * 8), *(LAS u32x2*)(smem + L_PARK + 32768 + wid * 1024 + 512 + lane * 8)};
#pragma unroll
    for (int u = 0; u < 2; ++u) {
      f32x4 g[9];
#pragma unroll
      for (int a = 0; a < 9; ++a) g[a] = acc[a][u] + *(LAS f32x4*)(src + (a * 2 + u) * 1024);
      const int tb = tm * 8 + 2 * wid + u, ho = (tb * 72 + 9 * pp + 4 * ee) * 1024;
#pragma unroll
      for (int c = 0; c < 4; ++c) { const u32x4 z = *(LAS u32x4*)(pk + (u * 4 + c) * 1024); u32x4 o;
        o[0] = pk2(bflo(z[0]) * g[2 * c][0], bfhi(z[0]) * g[2 * c][1]); o[1] = pk2(bflo(z[1]) * g[2 * c][2], bfhi(z[1]) * g[2 * c][3]);
        o[2] = pk2(bflo(z[2]) * g[2 * c + 1][0], bfhi(z[2]) * g[2 * c + 1][1]); o[3] = pk2(bflo(z[3]) * g[2 * c + 1][2], bfhi(z[3]) * g[2 * c + 1][3]);
        __builtin_amdgcn_raw_buffer_store_b128(o, rHs, v16, ho + c * 1024, 16); }
      u32x2 o; o[0] = pk2(bflo(y8[u][0]) * g[8][0], bfhi(y8[u][0]) * g[8][1]); o[1] = pk2(bflo(y8[u][1]) * g[8][2], bfhi(y8[u][1]) * g[8][3]);
      __builtin_amdgcn_raw_buffer_store_b64(o, rHs, ee * 512 + (fr + 16 * (fq >> 1)) * 16 + (fq & 1) * 8, (tb * 72 + 9 * pp + 8) * 1024, 16);
    }
  }
#pragma unroll
  for (int k = 0; k < 4; ++k) ul[k] = *(LAS u32x4*)(smem + L_US + wid * 4096 + lane * 64 + k * 16);
  WAITL0(); BAR();
  STAMP(10);
  if (ssm) {
    LAS char* cv = smem + wid * CV_W;
    LAS char* ub = cv + CV_U;
    {
#pragma unroll
      for (int k = 0; k < 4; ++k) { const u32x4 s = ul[3 - k]; u32x4 o;
#pragma unroll
        for (int m = 0; m < 4; ++m) o[m] = __builtin_amdgcn_alignbit(s[3 - m], s[3 - m], 16);
        *(LAS u32x4*)(ub + (15 + lane) * 96 + k * 16) = o;
        if (lane < 15) *(LAS u32x4*)(ub + lane * 96 + k * 16) = (u32x4){0, 0, 0, 0}; }
    }
    const int cb0 = 248, cb1 = 4608 + 64, cb2 = 2 * 4608 + 128, cb3 = 3 * 4608 + 192;
    {
      LAS u16* kc = (LAS u16*)(smem + L_KC + wid * 1344);
      auto emit = [&](int k, const unsigned (&pr)[6]) { const int o = 64 + 512 * k + 8 * lane;
        *(LAS u32x2*)(cv + cb0 + o) = (u32x2){pr[0], pr[2]}; *(LAS u32x2*)(cv + cb1 + o) = (u32x2){pr[1], pr[3]};
        *(LAS u32x2*)(cv + cb2 + o) = (u32x2){pr[2], pr[4]}; *(LAS u32x2*)(cv + cb3 + o) = (u32x2){pr[3], pr[5]}; };
      LAS char* kb = (LAS char*)kc;
      const u32x2 q0 = *(LAS u32x2*)(kb + 8 * lane), q1 = *(LAS u32x2*)(kb + 8 * lane + 8);
      const unsigned r0 = *(LAS unsigned*)(kb + 512 + 4 * lane), r1 = *(LAS unsigned*)(kb + 516 + 4 * lane);
      unsigned ab[6], cx[2];
#pragma unroll
      for (int k = 2; k < 4; ++k) { const int m = 256 + 64 * k + lane; ab[k - 2] = kc[m] | ((unsigned)kc[m + 1] << 16); cx[k - 2] = kc[m + 2]; }
#pragma unroll
      for (int k = 4; k < 8; ++k) { const int m = 384 + 32 * k + (lane >> 1); ab[k - 2] = kc[m] | ((unsigned)kc[m + 1] << 16); }
      const unsigned k0 = kc[0], k1 = kc[1], k2 = kc[2];
      const bool l63 = lane == 63;
      { const unsigned p45 = l63 ? __builtin_amdgcn_perm(q1[0], q1[0], 0x01000100u) : q1[0], p56 = l63 ? q1[0] : __builtin_amdgcn_alignbit(q1[1], q1[0], 16);
        const unsigned pr[6] = {q0[0], __builtin_amdgcn_alignbit(q0[1], q0[0], 16), q0[1], __builtin_amdgcn_alignbit(q1[0], q0[1], 16), p45, p56}; emit(0, pr); }
      { const unsigned v22 = __builtin_amdgcn_perm(r1, r1, 0x01000100u);
        const unsigned pr[6] = {__builtin_amdgcn_perm(r0, r0, 0x01000100u), r0, __builtin_amdgcn_alignbit(r1, r0, 16), v22, v22, l63 ? v22 : r1}; emit(1, pr); }
      { const bool od = lane & 1; const unsigned s1 = od ? 0x03020100u : 0x01000100u, s2 = od ? 0x03020302u : 0x03020100u, s5 = od ? 0x03020302u : 0x05040302u;
#pragma unroll
        for (int k = 2; k < 4; ++k) { const unsigned w = ab[k - 2], bb2 = __builtin_amdgcn_perm(w, w, 0x03020302u);
          const unsigned pr[6] = {__builtin_amdgcn_perm(w, w, 0x01000100u), __builtin_amdgcn_perm(w, w, s1), __builtin_amdgcn_perm(w, w, s2), bb2, bb2, __builtin_amdgcn_perm(cx[k - 2], w, s5)}; emit(k, pr); } }
      { const int na = (0x0415 >> (4 * (lane & 3))) & 15; unsigned s3[5];
#pragma unroll
        for (int n = 0; n < 5; ++n) s3[n] = (n < na ? 0x0100u : 0x0302u) | (n + 1 < na ? 0x01000000u : 0x03020000u);
#pragma unroll
        for (int k = 4; k < 8; ++k) { const unsigned w = ab[k - 2];
          const unsigned pr[6] = {__builtin_amdgcn_perm(w, w, s3[0]), __builtin_amdgcn_perm(w, w, s3[1]), __builtin_amdgcn_perm(w, w, s3[2]), __builtin_amdgcn_perm(w, w, s3[3]),
                                  __builtin_amdgcn_perm(w, w, s3[4]), __builtin_amdgcn_perm(w, w, 0x03020302u)}; emit(k, pr); } }
      if (lane < 8) { const bool l7 = lane == 7;
        *(LAS u32x2*)(cv + cb0 + 8 * lane) = (u32x2){0u, 0u};
        *(LAS u32x2*)(cv + cb1 + 8 * lane) = (u32x2){0u, l7 ? (k0 << 16) : 0u};
        *(LAS u32x2*)(cv + cb2 + 8 * lane) = (u32x2){0u, l7 ? (k0 | (k1 << 16)) : 0u};
        *(LAS u32x2*)(cv + cb3 + 8 * lane) = (u32x2){l7 ? (k0 << 16) : 0u, l7 ? (k1 | (k2 << 16)) : 0u}; }
    }
    WAITV(0);
    if (lane == 0) __hip_atomic_store((GAS unsigned*)ctl + C_F3 + tm * 64 + tn * 4 + wid, E, RLX);
    STAMP(11);
    STAMP(18);
    const int sg = (fr + 1) & 3;
    LAS char* pA = cv + (sg == 0 ? cb0 : sg == 1 ? cb1 : sg == 2 ? cb2 : cb3) + 2 * (8 * (fr >> 2) + 8 * fq + (((fr & 3) + 1) & 4));
    LAS char* pB = cv + CV_U + 96 * (15 + fr) + 16 * fq - 96 * 63;
    f32x4 ya[4][2];
#pragma unroll
    for (int c = 0; c < 4; ++c) { ya[c][0] = (f32x4){0, 0, 0, 0}; ya[c][1] = (f32x4){0, 0, 0, 0}; }
    {
      u32x4 ra[2][4][2]; bf16x8 rb[2][4];
      pB += 96 * 62;
      auto rdc = [&](int s, int kk) {
#pragma unroll
        for (int e = 0; e < 4; ++e) {
          rb[s][e] = *(LAS bf16x8*)(pB + 96 * (1 - kk) + 1536 * e);
          const u32x4 lo = *(LAS u32x4*)(pA + 64 * kk + 1024 * e); const u32x2 hi = *(LAS u32x2*)(pA + 64 * kk + 1024 * e + 16);
          ra[s][e][0] = lo; ra[s][e][1] = (u32x4){lo[2], lo[3], hi[0], hi[1]}; }
      };
      auto mmc = [&](int s) {
#pragma unroll
        for (int e = 0; e < 4; ++e)
#pragma unroll
          for (int c = e; c < 4; ++c) {
            ya[c][0] = __builtin_amdgcn_mfma_f32_16x16x32_bf16(__builtin_bit_cast(bf16x8, ra[s][e][0]), rb[s][c - e], ya[c][0], 0, 0, 0);
            ya[c][1] = __builtin_amdgcn_mfma_f32_16x16x32_bf16(__builtin_bit_cast(bf16x8, ra[s][e][1]), rb[s][c - e], ya[c][1], 0, 0, 0); }
#pragma unroll
        for (int i = 0; i < 12; ++i) { __builtin_amdgcn_sched_group_barrier(0x8, 1, 0); __builtin_amdgcn_sched_group_barrier(0x100, 1, 0); }
        __builtin_amdgcn_sched_group_barrier(0x8, 8, 0);
        SB0();
      };
      rdc(0, 0);
      SB0();
#pragma unroll 1
      for (int g = 0; g < 8; ++g) {
        rdc(1, 1); mmc(0);
        pA += 128; pB -= 192;
        rdc(0, 0); mmc(1);
      }
    }
    STAMP(19);
#pragma unroll
    for (int c = 0; c < 4; ++c) { const u32x4 uu = *(LAS u32x4*)(ub + 96 * (15 + 16 * c + fr) + 16 * (3 - fq)); u32x4 o;
      o[0] = pk2(ya[c][0][0] + bfhi(uu[3]) * dskf, ya[c][0][1] + bflo(uu[3]) * dskf); o[1] = pk2(ya[c][0][2] + bfhi(uu[2]) * dskf, ya[c][0][3] + bflo(uu[2]) * dskf);
      o[2] = pk2(ya[c][1][0] + bfhi(uu[1]) * dskf, ya[c][1][1] + bflo(uu[1]) * dskf); o[3] = pk2(ya[c][1][2] + bfhi(uu[0]) * dskf, ya[c][1][3] + bflo(uu[0]) * dskf);
      *(LAS u32x4*)(cv + c * 1024 + fr * 64 + fq * 16) = o; }
  } else {
    WAITV(0);
    if (lane == 0) __hip_atomic_store((GAS unsigned*)ctl + C_F3 + tm * 64 + tn * 4 + wid, E, RLX);
    STAMP(11);
  }
  STAMP(12);
  relane();
  constexpr int DEPTH = 3;
  {
    const rsrc_t rH = mkrs(ws + OFF_H + (size_t)t3 * 4 * 72 * 1024, 4 * 72 * 1024);
    const rsrc_t rF = mkrs(ws + OFF_WF + (size_t)dn * 72 * 4608, 72 * 4608);
    const int vh = 4096 + (fq * 8 + (fr & 7)) * 16;
    bf16x8 H[DEPTH][4], W[DEPTH][5];
    auto ldH = [&](int s, int j) { const int P = j * 4 + wid;
#pragma unroll
      for (int b = 0; b < 4; ++b) H[s][b] = __builtin_bit_cast(bf16x8, __builtin_amdgcn_raw_buffer_load_b128(rH, v16, P * 1024 + b * 72 * 1024, AUXH)); };
    auto ldW = [&](int s, int j) { const int P = j * 4 + wid;
      if (j < JL) { LAS char* p = smem + L_WPF + P * 4608;
#pragma unroll
        for (int c = 0; c < 4; ++c) W[s][c] = *(LAS bf16x8*)(p + v16 + c * 1024);
        W[s][4] = *(LAS bf16x8*)(p + vh);
      } else {
#pragma unroll
        for (int c = 0; c < 4; ++c) W[s][c] = __builtin_bit_cast(bf16x8, __builtin_amdgcn_raw_buffer_load_b128(rF, v16 + c * 1024, P * 4608, AUXW));
        W[s][4] = __builtin_bit_cast(bf16x8, __builtin_amdgcn_raw_buffer_load_b128(rF, vh, P * 4608, AUXW));
      } };
    WAITL0(); BAR();
    STAMP(13);
#pragma unroll
    for (int s = 0; s < DEPTH; ++s) { ldH(s, s); ldW(s, s); }
    SB0();
    f32x4 ac3[4][5];
#pragma unroll
    for (int b = 0; b < 4; ++b)
#pragma unroll
      for (int c = 0; c < 5; ++c) ac3[b][c] = (f32x4){0, 0, 0, 0};
#pragma unroll
    for (int j = 0; j < 18; ++j) {
      const int s = j % DEPTH;
#pragma unroll
      for (int b = 0; b < 4; ++b)
#pragma unroll
        for (int c = 0; c < 5; ++c) ac3[b][c] = __builtin_amdgcn_mfma_f32_16x16x32_bf16(H[s][b], W[s][c], ac3[b][c], 0, 0, 0);
      if (j == PROGJ && wid == 0 && lane == 0) *(volatile LAS unsigned*)(smem + L_PROG) = 1u;
      if (j + DEPTH < 18) { ldH(s, j + DEPTH); ldW(s, j + DEPTH); }
      SB0();
    }
    STAMP(14);
    WAITL0(); BAR();
    f32x4 mine[5];
#pragma unroll
    for (int b = 0; b < 4; ++b) {
      if (b == wid) {
#pragma unroll
        for (int c = 0; c < 5; ++c) mine[c] = ac3[b][c];
      } else {
        LAS char* dst = smem + (b * 3 + ((wid - b - 1) & 3)) * 5120 + v16;
#pragma unroll
        for (int c = 0; c < 5; ++c) *(LAS f32x4*)(dst + c * 1024) = ac3[b][c];
      }
    }
    WAITL0(); BAR();
    STAMP(21);
    LAS char* src = smem + wid * 3 * 5120 + v16;
    const f32x4 gv = *(LAS f32x4*)(smem + L_AT + 9216 + (wid * 16 + fq * 4) * 4);
    const int t0 = t3 * 64 + wid * 16 + fq * 4;
#pragma unroll
    for (int c = 0; c < 5; ++c) {
      const f32x4 v = mine[c] + *(LAS f32x4*)(src + c * 1024) + (*(LAS f32x4*)(src + 5120 + c * 1024) + *(LAS f32x4*)(src + 10240 + c * 1024));
      const u32x2 av = *(LAS u32x2*)(smem + L_AT + (c * 16 + (c < 4 ? fr : (fr & 7))) * 128 + (wid * 16 + fq * 4) * 2);
      const unsigned p0 = pk2(gv[0] * bflo(av[0]) + (1.f - gv[0]) * v[0], gv[1] * bfhi(av[0]) + (1.f - gv[1]) * v[1]);
      const unsigned p1 = pk2(gv[2] * bflo(av[1]) + (1.f - gv[2]) * v[2], gv[3] * bfhi(av[1]) + (1.f - gv[3]) * v[3]);
      if (c < 4 || fr < 8) { u16* o = out + (size_t)t0 * 576 + dn * 72 + c * 16 + fr;
        o[0] = (u16)p0; o[576] = (u16)(p0 >> 16); o[1152] = (u16)p1; o[1728] = (u16)(p1 >> 16); }
    }
  }
  STAMP(15);
}

extern "C" void kernel_launch(void* const* d_in, const int* in_sizes, int n_in, void* d_out, int out_size, void* d_ws, size_t ws_size, hipStream_t stream) {
  (void)in_sizes; (void)n_in; (void)out_size; (void)ws_size;
  (void)hipFuncSetAttribute((const void*)gmix_fused, hipFuncAttributeMaxDynamicSharedMemorySize, LDS_BYTES);
  (void)hipMemsetAsync((char*)d_ws + OFF_ST, 0, WS_END - OFF_ST, stream);
  gmix_fused<<<256, 512, LDS_BYTES, stream>>>((const u16*)d_in[0], (const u16*)d_in[1], (const u16*)d_in[2], (const u16*)d_in[3], (const u16*)d_in[4], (const u16*)d_in[5],
      (const u16*)d_in[6], (const u16*)d_in[8], (const u16*)d_in[9], (const u16*)d_in[10], (const u16*)d_in[11], (const u16*)d_in[12], (u16*)d_out, (char*)d_ws
  );
}
```

```cpp
#include <hip/hip_runtime.h>
#include <hip/hip_bf16.h>

#define LAS __attribute__((address_space(3)))
#define GAS __attribute__((address_space(1)))
typedef unsigned short u16;
typedef unsigned u32x4 __attribute__((ext_vector_type(4)));
typedef unsigned u32x2 __attribute__((ext_vector_type(2)));
typedef short bf16x8 __attribute__((ext_vector_type(8)));
typedef short s16x4 __attribute__((ext_vector_type(4)));
typedef _Float16 f16x8 __attribute__((ext_vector_type(8)));
typedef _Float16 f16x2 __attribute__((ext_vector_type(2)));
typedef __bf16 bfx2 __attribute__((ext_vector_type(2)));
typedef float f32x4 __attribute__((ext_vector_type(4)));
typedef float f32x2 __attribute__((ext_vector_type(2)));

constexpr int SZ_F = 9437184;
constexpr int OFF_Z2 = 0, OFF_H = SZ_F, OFF_WF = 2 * SZ_F, OFF_XT = OFF_WF + 2654208, OFF_AT = OFF_XT + 2359296,
              OFF_GATE = OFF_AT + 2359296, OFF_ST = OFF_GATE + 8192, OFF_CTL = OFF_ST + 524288, WS_END = OFF_CTL + 32768;
constexpr int C_R = 0, C_S = 256, C_A = 768, C_F2 = 1024, C_F3 = 2048, C_FW = 3072, C_FA = 3584;
constexpr int ZST = 40960, GST = 34816, L_PARK = 104448, L_KG = 122880, L_PS = 122880, L_RN = 124928, L_RT = 125952, L_KC = 157696, L_AT = 146688, L_US = 73728,
              L_PROG = 163072, L_EPO = 163076, L_BIAS = 163328, L_DUMP = 163584, LDS_BYTES = 163840;
constexpr int CV_W = 25856, CV_U = 18176, L_WPF = 103424, JL = 2;
#define HPRIO 2
#define LPRIO 3
#define PROGJ 11
#define HS 2
#define FS 7
#define TMODE 1
#define WSPT 1
constexpr unsigned SPIN_CAP = 1u << 22;

#define AUXH 16
#define AUXW 16
#define BAR() do { asm volatile("" ::: "memory"); __builtin_amdgcn_s_barrier(); asm volatile("" ::: "memory"); } while (0)
#define WAITV(n) asm volatile("s_waitcnt vmcnt(%0)" ::"i"(n) : "memory")
#define WAITL0() do { __builtin_amdgcn_s_waitcnt(0xC07F); asm volatile("" ::: "memory"); } while (0)
#define SB0() __builtin_amdgcn_sched_barrier(0)
#define RLX __ATOMIC_RELAXED, __HIP_MEMORY_SCOPE_AGENT
#define STAMP(k)
typedef __amdgpu_buffer_rsrc_t rsrc_t;
__device__ __forceinline__ rsrc_t mkrs(const void* p, int bytes) { return __builtin_amdgcn_make_buffer_rsrc((void*)p, 0, bytes, 0x00020000); }
__device__ __forceinline__ void dma16(rsrc_t r, LAS char* dst, int voff, int soff) { __builtin_amdgcn_raw_ptr_buffer_load_lds(r, (LAS void*)dst, 16, voff, soff, 0, 0); }
__device__ __forceinline__ void dma16c(rsrc_t r, LAS char* dst, int voff, int soff) { __builtin_amdgcn_raw_ptr_buffer_load_lds(r, (LAS void*)dst, 16, voff, soff, 0, 16); }
__device__ __forceinline__ unsigned pk2(float lo, float hi) { f32x2 v = {lo, hi}; return __builtin_bit_cast(unsigned, __builtin_convertvector(v, bfx2)); }
__device__ __forceinline__ float bflo(unsigned v) { return __uint_as_float(v << 16); }
__device__ __forceinline__ float bfhi(unsigned v) { return __uint_as_float(v & 0xffff0000u); }
__device__ __forceinline__ float bf2f(u16 v) { return __uint_as_float(((unsigned)v) << 16); }
__device__ __forceinline__ float rbf(float f) { return bflo(pk2(f, f)); }
__device__ __forceinline__ f32x2 gelu2(f32x2 v) {
  const f32x2 p = (v * v) * (-0.10294324f) + (-2.3022082f);
  const f32x2 a = v * p;
  f32x2 d; d.x = __builtin_amdgcn_exp2f(a.x); d.y = __builtin_amdgcn_exp2f(a.y);
  d = d + 1.0f;
  f32x2 r; r.x = __builtin_amdgcn_rcpf(d.x); r.y = __builtin_amdgcn_rcpf(d.y);
  return v * r;
}
template <int CTRL> __device__ __forceinline__ float dppadd(float x) { return x + __builtin_amdgcn_update_dpp(0.f, x, CTRL, 0xf, 0xf, true); }
template <int CTRL> __device__ __forceinline__ float dppmax(float x) { return fmaxf(x, __builtin_amdgcn_update_dpp(0.f, x, CTRL, 0xf, 0xf, true)); }
__device__ __forceinline__ float rowsum16(float x) { x = dppadd<0x128>(x); x = dppadd<0x124>(x); x = dppadd<0x122>(x); x = dppadd<0x121>(x); return x; }
template <int SLP = 1>
__device__ __forceinline__ void wait_flags(const unsigned* f, bool act, unsigned E) {
  for (unsigned sp = 0; sp < SPIN_CAP; ++sp) {
    const unsigned v = __hip_atomic_load((GAS unsigned*)f, RLX);
    if (__all(!act || v == E)) break;
    __builtin_amdgcn_s_sleep(SLP);
  }
  asm volatile("" ::: "memory");
}
template <int AUX> __device__ __forceinline__ void touch_(LAS char* smem, const void* p) { __builtin_amdgcn_global_load_lds((const GAS unsigned*)p, (LAS unsigned*)(smem + L_DUMP), 4, 0, AUX); }
__device__ __forceinline__ void touch_asm(const void* p) { unsigned keep;
  asm volatile("s_mov_b32 %0, m0\n\ts_mov_b32 m0, %2\n\ts_nop 0\n\tglobal_load_lds_dword %1, off\n\ts_mov_b32 m0, %0" : "=&s"(keep) : "v"(p), "s"((unsigned)L_DUMP) : "memory"); }
#define touch(p) touch_<0>(smem, p)
#define touchc(p) touch_<16>(smem, p)

__device__ __forceinline__ float fmul_(float a, float b) { float r; asm("v_mul_f32 %0, %1, %2" : "=v"(r) : "v"(a), "v"(b)); return r; }
__device__ __forceinline__ float ffma_(float a, float b, float c) { float r; asm("v_fma_f32 %0, %1, %2, %3" : "=v"(r) : "v"(a), "v"(b), "v"(c)); return r; }
__device__ __forceinline__ float fnma_(float a, float b, float c) { float r; asm("v_fma_f32 %0, -%1, %2, %3" : "=v"(r) : "v"(a), "v"(b), "v"(c)); return r; }
__device__ __forceinline__ f32x2 cmul(f32x2 a, f32x2 b) { return (f32x2){fnma_(a.y, b.y, fmul_(a.x, b.x)), ffma_(a.y, b.x, fmul_(a.x, b.y))}; }
__device__ __forceinline__ f32x2 cmlc(f32x2 a, f32x2 b) { return (f32x2){ffma_(a.y, b.y, fmul_(a.x, b.x)), fnma_(a.x, b.y, fmul_(a.y, b.x))}; }
__device__ __forceinline__ float ssm_par(const u32x4 a, unsigned ldtv, int h, f32x4& p) {
  const float dt = rbf(__expf(__uint_as_float(ldtv << 16)));
  float mx = fmaxf(fmaxf(fabsf(bflo(a[2])), fabsf(bfhi(a[2]))), fmaxf(fabsf(bflo(a[3])), fabsf(bfhi(a[3]))));
  mx = dppmax<0x128>(mx); mx = dppmax<0x124>(mx); mx = dppmax<0x122>(mx); mx = dppmax<0x121>(mx);
  const unsigned mb = __float_as_uint(mx);
  const float mw = fmaxf(fmaxf(__uint_as_float(__builtin_amdgcn_readlane(mb, 0)), __uint_as_float(__builtin_amdgcn_readlane(mb, 16))),
                         fmaxf(__uint_as_float(__builtin_amdgcn_readlane(mb, 32)), __uint_as_float(__builtin_amdgcn_readlane(mb, 48))));
  const int ex = (mw > 0.f && mw < 3.0e38f) ? __builtin_amdgcn_frexp_expf(mw) : 0;
  const int sh = h ? 0 : 16;
  const float la = bfhi(a[0] << sh), ai = bfhi(a[1] << sh), cr = bfhi(a[2] << sh), ci = bfhi(a[3] << sh);
  p = (f32x4){-rbf(__expf(la)) * dt * 1.44269504f, ai * dt * 0.15915494f, __builtin_amdgcn_ldexpf(cr, -ex), __builtin_amdgcn_ldexpf(ci, -ex)};
  return __builtin_amdgcn_ldexpf(2.f * dt, ex);
}
__device__ __forceinline__ f16x2 tohf(f32x2 v) { return __builtin_convertvector(v, f16x2); }
__device__ __forceinline__ f16x2 hmul(f16x2 a, f16x2 b) { return __builtin_elementwise_fma((f16x2){-b.y, b.x}, (f16x2){a.y, a.y}, b * (f16x2){a.x, a.x}); }
__device__ __forceinline__ f16x2 hmlc(f16x2 a, f16x2 b) { return __builtin_elementwise_fma((f16x2){a.y, -a.x}, (f16x2){b.y, b.y}, a * (f16x2){b.x, b.x}); }
template <int TS>
__device__ __forceinline__ void ssm_kgen(const f32x4 p, LAS char* kb, int lane, f32x4 (&kd)[3]) {
  int tb = 0;
  auto put = [&](int row, f16x2 v) { *(LAS f16x2*)(kb + tb + row * 272 + lane * 4) = v; };
  LAS char* rp = kb + (lane & 15) * 272 + (lane >> 4) * 16;
  f32x2 w[12];
#pragma unroll
  for (int k = 0; k < 12; ++k) {
    if (k % 3 == 0) { const float sc = (float)(1 << k), e = __builtin_amdgcn_exp2f(p[0] * sc), ph = __builtin_amdgcn_fractf(p[1] * sc);
      w[k] = (f32x2){e * __builtin_amdgcn_cosf(ph), e * __builtin_amdgcn_sinf(ph)}; }
    else { const f32x2 v = w[k - 1]; w[k] = (f32x2){fnma_(v.y, v.y, fmul_(v.x, v.x)), fmul_(v.x + v.x, v.y)}; } }
  f32x2 y[8];
  y[0] = (f32x2){p[2], -p[3]}; y[1] = cmlc(y[0], w[3]); y[2] = cmlc(y[0], w[4]); y[3] = cmlc(y[1], w[4]);
#pragma unroll
  for (int b = 0; b < 4; ++b) y[4 + b] = cmlc(y[b], w[5]);
  const f16x2 h6 = tohf(w[6]), h7 = tohf(w[7]), h10 = tohf(w[10]);
#pragma unroll
  for (int b = 0; b < 8; ++b) { const f16x2 hy = tohf(y[b]); put(b, hy); put(8 + b, hmlc(hy, h6)); }
  f16x8 fb[4];
  auto rdb = [&]() {
#pragma unroll
    for (int k = 0; k < 4; ++k) fb[k] = *(LAS f16x8*)(rp + k * 64); };
  auto tile_ = [&](int t) { kd[t] = (f32x4){0, 0, 0, 0};
#pragma unroll
    for (int k = 0; k < 4; ++k) kd[t] = __builtin_amdgcn_mfma_f32_16x16x32_f16(*(LAS f16x8*)(rp + (t + 1) * TS + k * 64), fb[k], kd[t], 0, 0, 0); };
  auto tile = [&](int t) { if (TS == 0) tile_(t); tb += TS; };
  if (TS == 0) rdb();
  tb += TS;
  f16x2 g[8];
  { const f32x2 g3 = cmul(w[0], w[1]);
    g[0] = (f16x2){(_Float16)1.f, (_Float16)0.f}; g[1] = tohf(w[0]); g[2] = tohf(w[1]); g[3] = tohf(g3); g[4] = tohf(w[2]);
    g[5] = tohf(cmul(w[2], w[0])); g[6] = tohf(cmul(w[2], w[1])); g[7] = tohf(cmul(w[2], g3)); }
#pragma unroll
  for (int c = 0; c < 8; ++c) { put(c, g[c]); put(8 + c, c ? hmul(g[c], h7) : h7); }
  tile(0);
  f16x2 f[8];
  { const f32x2 f3 = cmul(w[8], w[7]);
    f[1] = h7; f[2] = tohf(w[8]); f[3] = tohf(f3); f[4] = tohf(w[9]); f[5] = tohf(cmul(w[9], w[7])); f[6] = tohf(cmul(w[9], w[8])); f[7] = tohf(cmul(w[9], f3)); }
#pragma unroll
  for (int a = 0; a < 2; ++a)
#pragma unroll
    for (int c = 0; c < 4; ++c) put(4 * a + c, c ? hmul(f[2 + a], g[2 * c]) : f[2 + a]);
#pragma unroll
  for (int a = 0; a < 4; ++a) { put(8 + 2 * a, f[4 + a]); put(9 + 2 * a, hmul(f[4 + a], g[4])); }
  tile(1);
#pragma unroll
  for (int a = 0; a < 8; ++a) put(a, a ? hmul(h10, f[a]) : h10);
  put(8, tohf(w[11]));
  tile(2);
  if (TS) { SB0(); rdb(); tile_(0); tile_(1); tile_(2); }
}

struct ZF { bf16x8 x[4]; s16x4 w[5][2]; };

__global__ __launch_bounds__(512, 2) void gmix_fused(const u16* __restrict__ x, const u16* __restrict__ lar, const u16* __restrict__ aim, const u16* __restrict__ cre,
    const u16* __restrict__ cim, const u16* __restrict__ ldt, const u16* __restrict__ dsk, const u16* __restrict__ win, const u16* __restrict__ wsp,
    const u16* __restrict__ bsp, const u16* __restrict__ wout, const u16* __restrict__ gw, u16* __restrict__ out, char* ws
) {
  extern __shared__ __attribute__((aligned(16))) char smem_[];
  LAS char* smem = (LAS char*)smem_;
  const int wid = __builtin_amdgcn_readfirstlane(threadIdx.x >> 6);
  int lane = threadIdx.x & 63;
  const int bx = blockIdx.x & 7, bl = blockIdx.x >> 3;
  const int tm = (bx >> 2) * 8 + (bl & 7), tn = (bx & 3) * 4 + (bl >> 3);
  const int pp = tn >> 1, ee = tn & 1;
  const int t3 = bx * 4 + (bl & 3), dn = bl >> 2;
  unsigned* ctl = (unsigned*)(ws + OFF_CTL);
  if (wid != 7) STAMP(0);
  int fr = lane & 15, fq = lane >> 4, v16 = lane * 16;
  const bool ssm = tm < 9;
  const int wcol = (288 * tn) & ~127;
  const int uP = (tm - 9) * 16 + tn;
  u32x4 sj[6][2]; unsigned pdt2 = 0;
  const bool isL = wid == 4 || wid == 5, gjob = !ssm && uP < 72 && !isL;
  const int grow = (uP * 6 + (wid < 4 ? wid : wid - 2)) * 5;
  auto jobload = [&](int jb) {
    if (ssm) { const int d = 36 * tn + 4 * tm + (jb & 3), o = d * 64 + lane;
      sj[0][0] = (u32x4){((const unsigned*)lar)[o], ((const unsigned*)aim)[o], ((const unsigned*)cre)[o], ((const unsigned*)cim)[o]};
      pdt2 = (((const unsigned*)ldt)[d >> 1] >> (16 * (d & 1))) & 0xffffu;
    } else if (gjob) {
#pragma unroll
      for (int k = 0; k < 2; ++k) { const int c = k ? 64 + (lane & 7) : lane; sj[5][k] = *(const u32x4*)(gw + c * 8);
#pragma unroll
        for (int r = 0; r < 5; ++r) sj[r][k] = *(const u32x4*)(x + (size_t)(grow + r < 2048 ? grow + r : 2047) * 576 + c * 8); }
    } };
  unsigned nonce = 0;
  if (!isL) {
    nonce = __builtin_amdgcn_raw_buffer_load_b32(mkrs(ctl, wid == 7 ? 1024 : 0), 0, (C_R + blockIdx.x) * 4, 16);
    jobload(wid);
#pragma unroll
    for (int k = 0; k < 6; ++k) asm volatile("" : "+v"(sj[k][0]), "+v"(sj[k][1]));
    asm volatile("" : "+v"(nonce));
    ++nonce;
    if (wid == 7 && lane == 0) __hip_atomic_store((GAS unsigned*)ctl + C_R + blockIdx.x, nonce, RLX);
  }
  else if (TMODE != 2) {
    const int d = ssm ? 36 * tn + 4 * tm + (wid & 3) : 0, o = d * 64 + lane;
    unsigned a0, a1, a2, a3;
    asm volatile("global_load_dword %0, %4, off\n\tglobal_load_dword %1, %5, off\n\tglobal_load_dword %2, %6, off\n\tglobal_load_dword %3, %7, off"
                 : "=&v"(a0), "=&v"(a1), "=&v"(a2), "=&v"(a3)
                 : "v"((const unsigned*)lar + o), "v"((const unsigned*)aim + o), "v"((const unsigned*)cre + o), "v"((const unsigned*)cim + o) : "memory");
    const bool isx = lane < 32 || lane >= 56; const int l3 = (lane - 32) / 3;
    const char* tsh = isx ? (const char*)x + (size_t)(tm * 128 + (bl >> 3) * 32 + (lane & 31)) * 1152 + (wid & 1) * 128
                          : (const char*)win + (size_t)(64 * (wid & 1) + 8 * (bl & 7) + l3) * 9216 + wcol + 4608 + (lane - 32 - 3 * l3) * 128;
    const int tst = isx ? 256 : 128 * 9216;
#pragma unroll
    for (int k = 0; k < 5; ++k) touch_asm(tsh + (size_t)(k == 4 && (wid & 1) ? 3 : k) * tst);
    asm volatile("s_waitcnt vmcnt(5)" : "+v"(a0), "+v"(a1), "+v"(a2), "+v"(a3) :: "memory");
    sj[0][0] = (u32x4){a0, a1, a2, a3};
    pdt2 = (((const unsigned*)ldt)[d >> 1] >> (16 * (d & 1))) & 0xffffu;
  }
  const int lw = wid - 4;
  const rsrc_t rX = mkrs(x + (size_t)tm * 128 * 576, 128 * 1152);
  const rsrc_t rWi = mkrs(win, 576 * 9216);
  int vxP[2], vxN[2], vw[6];
  auto zaddr = [&](int ln) {
#pragma unroll
    for (int h = 0; h < 2; ++h) {
      const int swz = ((ln & 7) ^ ((4 * h + (ln >> 4)) & 7)) << 4;
      const int i = 8 * h + (ln >> 3);
      vxP[h] = (8 * (i >> 2) + (i & 3)) * 1152 + swz;
      vxN[h] = i * 1152 + swz;
    }
#pragma unroll
    for (int q = 0; q < 6; ++q) { const int o = q * 1024 + ln * 16, row = o / 384, pos = (o - row * 384) >> 4; vw[q] = row * 9216 + ((pos ^ ((row & 2) | ((row & 8) >> 1))) << 4); }
  };
  auto relane = [&]() { int z = 0; asm volatile("" : "+v"(z));
    lane = __builtin_amdgcn_mbcnt_hi(~0u, __builtin_amdgcn_mbcnt_lo(~0u, z)); fr = lane & 15; fq = lane >> 4; v16 = lane * 16; };
  zaddr(lane);
  auto issueZ = [&](int g, int slot) {
    const bool z2 = g < 9; const int kt = z2 ? g : g - 9;
    LAS char* dst = smem + slot;
    const int sw = kt * 64 * 9216 + wcol + (z2 ? 4608 : 0);
    if (lw == 0) {
      const int v0 = z2 ? vxP[0] : vxN[0], v1 = z2 ? vxP[1] : vxN[1], odd = (z2 ? 4 : 16) * 1152;
#pragma unroll 1
      for (int t2 = 0; t2 < 4; ++t2) { const int so = t2 * 32 * 1152 + kt * 128; LAS char* d = dst + t2 * 4096;
        dma16(rX, d, v0, so); dma16(rX, d + 1024, v1, so); dma16(rX, d + 2048, v0, so + odd); dma16(rX, d + 3072, v1, so + odd); }
#pragma unroll
      for (int q = 0; q < 4; ++q) dma16(rWi, dst + 16384 + q * 1024, vw[q], sw);
    } else {
      dma16(rWi, dst + 16384 + 4096, vw[4], sw); dma16(rWi, dst + 16384 + 5120, vw[5], sw);
#pragma unroll 1
      for (int q6 = 1; q6 < 4; ++q6)
#pragma unroll
        for (int q = 0; q < 6; ++q) dma16(rWi, dst + 16384 + (q6 * 6 + q) * 1024, vw[q], sw + q6 * 16 * 9216);
    }
  };
  if (isL && LPRIO) __builtin_amdgcn_s_setprio(LPRIO);
  if (TMODE == 2 && isL) {
#pragma unroll 1
    for (int g = 0; g < 2; ++g) issueZ(g, g * ZST);
  }
  f32x4 kd[3]; float ksc = 0.f;
  if (TMODE != 2 || !isL) {
#pragma unroll 1
    for (int jb = wid;; jb -= 2) {
      if (ssm) {
        f32x4 p; ksc = ssm_par(sj[0][0], pdt2, jb >> 2, p);
        if (TMODE == 2) ssm_kgen<0>(p, smem + L_KG + jb * 4352, lane, kd);
        else ssm_kgen<4352>(p, smem + (jb < 7 ? jb * 17408 : L_KG + 12288), lane, kd);
        if (jb >= 4) {
#pragma unroll
          for (int t = 0; t < 3; ++t) *(LAS f32x4*)(smem + L_KG + (TMODE == 2 ? jb * 4352 : (jb - 4) * 3072) + t * 1024 + v16) = kd[t];
        }
      } else if (gjob) {
        float sv4 = 0.f;
#pragma unroll
        for (int r = 0; r < 5; ++r) { float s0 = 0.f, s1 = 0.f;
#pragma unroll
          for (int j = 0; j < 4; ++j) { s0 += bflo(sj[r][0][j]) * bflo(sj[5][0][j]) + bfhi(sj[r][0][j]) * bfhi(sj[5][0][j]); s1 += bflo(sj[r][1][j]) * bflo(sj[5][1][j]) + bfhi(sj[r][1][j]) * bfhi(sj[5][1][j]); }
          s0 += lane < 8 ? s1 : 0.f;
          s0 = rowsum16(s0); s0 += __shfl_xor(s0, 16); s0 += __shfl_xor(s0, 32);
          sv4 = lane == r ? s0 : sv4; }
        if (lane < 5 && grow + lane < 2048) __hip_atomic_store((GAS unsigned*)(ws + OFF_GATE) + grow + lane, __float_as_uint(__builtin_amdgcn_rcpf(1.f + __builtin_amdgcn_exp2f(-1.44269504f * sv4))), RLX);
      }
      if (TMODE != 2 || jb < 6 || !ssm) break;
      jobload(jb - 2);
    }
  }
  if (TMODE != 2) { WAITL0(); BAR(); }

  if (wid >= 6) {
    relane();
    const int hw = wid - 6;
    const bool retile = !ssm && uP < 72;
    if (hw == 0) { __builtin_amdgcn_global_load_lds((const GAS unsigned*)(bsp + tm * 128 + 2 * lane), (LAS unsigned*)(smem + L_BIAS), 4, 0, 0);
      if (lane == 0) *(LAS unsigned*)(smem + L_PROG) = 0u; }
    WAITL0();
    nonce = __builtin_amdgcn_readfirstlane(nonce);
    const bool leader = blockIdx.x == 0 && hw == 1;
    const rsrc_t rC = mkrs(ctl, 4096);
    auto echo = [&](const u32x4 r) {
      __builtin_amdgcn_raw_buffer_store_b128((u32x4){r[0], nonce, r[1], nonce}, rC, lane * 32, C_S * 4, 16);
      __builtin_amdgcn_raw_buffer_store_b128((u32x4){r[2], nonce, r[3], nonce}, rC, lane * 32 + 16, C_S * 4, 16); };
#pragma unroll 1
    for (int g = -1; g < 3; ++g) BAR();
    if (leader) { const u32x4 r = __builtin_amdgcn_raw_buffer_load_b128(rC, v16, C_R * 4, 16); SB0(); BAR(); BAR(); SB0(); echo(r); } else { WAITV(0); BAR(); BAR(); }
#pragma unroll 1
    for (int g = 5; g < 9; ++g) BAR();
    if (hw == 1) {
      unsigned ep = nonce;
      if (!leader) {
        for (unsigned sp = 0; sp < SPIN_CAP; ++sp) {
          const unsigned long long v = __hip_atomic_load((GAS unsigned long long*)(ctl + C_S) + blockIdx.x, RLX);
          ep = (unsigned)(v >> 32);
          if ((unsigned)v == nonce) break;
          __builtin_amdgcn_s_sleep(2);
        }
      }
      if (lane == 0) { *(LAS unsigned*)(smem + L_EPO) = ep; __hip_atomic_store((GAS unsigned*)ctl + C_A + blockIdx.x, ep, RLX); }
      WAITL0();
    }
    const int p9 = uP / 9, r9 = uP - p9 * 9; const bool nat = r9 == 8;
    LAS char* rs = smem + L_RT + hw * 18432;
    if (retile) {
      const rsrc_t rO = mkrs(wout, 2304 * 1152);
#pragma unroll 2
      for (int k = 0; k < 18; ++k) { const int c = k * 64 + lane, row = c / 36, ch = c - row * 36;
        const int f = nat ? 144 * (2 * p9 + (row >> 4)) + 128 + (row & 15) : 144 * (2 * p9 + (r9 >> 2)) + 32 * (r9 & 3) + row;
        dma16(rO, rs + k * 1024, f * 1152 + hw * 576 + ch * 16, 0); }
    }
    {
      const char* wv = (const char*)win + (size_t)(192 + (bl & 7) * 48 + hw * 24 + (lane >> 2)) * 9216 + wcol + ((lane & 3) < 3 ? (lane & 3) : 2) * 128;
      const char* sv = (const char*)wsp + (size_t)(blockIdx.x * 2 + hw) * 16384 + lane * 128;
      touch(wv); if (lane < 32) touch(wv + 16 * 9216);
      if (WSPT) { touch(sv); touch(sv + 8192); } }
    if (leader) {
      BAR(); BAR();
      u32x4 r = __builtin_amdgcn_raw_buffer_load_b128(rC, v16, C_R * 4, 16), a = __builtin_amdgcn_raw_buffer_load_b128(rC, v16, C_A * 4, 16);
      SB0(); BAR(); SB0();
      for (unsigned sp = 0; sp < SPIN_CAP; ++sp) {
        if (__all(a[0] == nonce && a[1] == nonce && a[2] == nonce && a[3] == nonce)) break;
        echo(r);
        __builtin_amdgcn_s_sleep(2);
        r = __builtin_amdgcn_raw_buffer_load_b128(rC, v16, C_R * 4, 16); a = __builtin_amdgcn_raw_buffer_load_b128(rC, v16, C_A * 4, 16);
      }
    } else if (!retile) { BAR(); BAR(); BAR(); }
    if (!retile) {
#pragma unroll 1
      for (int g = 12; g < 18; ++g) BAR();
    }
    if (retile) {
      BAR(); BAR(); BAR();
      WAITV(0);
      const int q4 = fr >> 2, p4 = lane & 3;
      const rsrc_t rF = mkrs(ws + OFF_WF, 2654208);
#pragma unroll 1
      for (int dl = 0; dl < 4; ++dl) {
#pragma unroll
        for (int c = 0; c < 5; ++c) { s16x4 h[2];
#pragma unroll
          for (int t = 0; t < 2; ++t) { const int row = nat ? 8 * fq + 4 * t + q4 : 16 * t + 4 * fq + q4; int col = 72 * dl + 16 * c + 4 * p4; col = col > 284 ? 284 : col;
            h[t] = __builtin_amdgcn_ds_read_tr16_b64_v4i16((LAS s16x4*)(rs + row * 576 + col * 2)); }
          const u32x4 v = __builtin_bit_cast(u32x4, __builtin_shufflevector(h[0], h[1], 0, 1, 2, 3, 4, 5, 6, 7));
          const int so = ((4 * hw + dl) * 72 + uP) * 4608 + c * 1024;
          if (c < 4) __builtin_amdgcn_raw_buffer_store_b128(v, rF, v16, so, 16);
          else if (fr < 8) __builtin_amdgcn_raw_buffer_store_b128(v, rF, (fq * 8 + fr) * 16, so, 16);
        }
        WAITL0(); BAR();
      }
      BAR(); BAR(); BAR(); BAR();
      WAITV(0);
      if (lane == 0) __hip_atomic_store((GAS unsigned*)ctl + C_FW + uP * 2 + hw, *(volatile LAS unsigned*)(smem + L_EPO), RLX);
    }
    STAMP(4);
    return;
  }

  if (wid >= 4) {
    relane(); zaddr(lane);
    {
      if (TMODE != 2) { WAITV(0); STAMP(3);
#pragma unroll 1
        for (int g = 0; g < 2; ++g) issueZ(g, g * ZST); }
      WAITV(20);
      STAMP(6);
      BAR();
      issueZ(2, 2 * ZST);
      int zs = 0;
#pragma unroll 1
      for (int g = 0; g < 15; ++g) { WAITV(20); BAR(); issueZ(g + 3, zs); zs = (zs == 2 * ZST) ? 0 : zs + ZST; }
    }
    const unsigned E = __builtin_amdgcn_readfirstlane(*(volatile LAS unsigned*)(smem + L_EPO));
    const rsrc_t rW = mkrs(wsp + (size_t)tm * 128 * 2048, 128 * 4096);
    const rsrc_t rZ = mkrs(ws + OFF_Z2, SZ_F);
    const int vW0 = (lane >> 3) * 4096 + (((lane & 7) ^ (lane >> 4)) << 4), vW1 = (lane >> 3) * 4096 + (((lane & 7) ^ (4 + (lane >> 4))) << 4);
    auto issueW = [&](int kt, int slot) {
      LAS char* dst = smem + slot * GST;
#pragma unroll
      for (int p = 0; p < 16; ++p) dma16(rW, dst + p * 1024, (p & 1) ? vW1 : vW0, p * 8 * 4096 + kt * 128);
    };
    auto issueZ2 = [&](int kt, int slot) {
      LAS char* dst = smem + slot * GST + 16384; const int so = (kt * 144 + 9 * tn) * 2048;
#pragma unroll
      for (int p = 0; p < 18; ++p) dma16c(rZ, dst + p * 1024, v16, so + p * 1024);
    };
    if (lw == 0) {
      WAITV(20); BAR(); issueW(0, 0);
      WAITV(16); BAR(); issueW(1, 1);
      BAR(); issueW(2, 2);
      WAITV(32); BAR();
      int slot = 0;
#pragma unroll 1
      for (int i = 0; i < 29; ++i) { WAITV(16); BAR(); issueW(i + 3, slot); slot = (slot == 2) ? 0 : slot + 1; }
      WAITV(16); BAR();
      if (ssm) {
        const rsrc_t rU = mkrs(ws + OFF_XT + (size_t)tn * 64 * 2304, 64 * 2304);
#pragma unroll 4
        for (int p = 0; p < 16; ++p) dma16c(rU, smem + L_US + p * 1024, (lane >> 2) * 2304 + (lane & 3) * 16, (p & 3) * 16 * 2304 + (4 * tm + (p >> 2)) * 64);
        WAITV(16);
      } else WAITV(0);
      BAR();
      BAR();
    } else {
      WAITV(20); BAR();
      WAITV(0); BAR();
      BAR();
      wait_flags(ctl + C_F2 + tn * 64 + lane, true, E);
      STAMP(1);
#pragma unroll 1
      for (int i = 0; i < 3; ++i) issueZ2(i, i);
      WAITV(36); BAR();
      int slot = 0;
#pragma unroll 1
      for (int i = 0; i < 29; ++i) { WAITV(18); BAR(); issueZ2(i + 3, slot); slot = (slot == 2) ? 0 : slot + 1; }
      WAITV(18); BAR();
      WAITV(0); BAR();
      BAR();
    }
    BAR();
    WAITV(0); BAR();
    BAR();
    if (lw == 0) {
      for (int k = 0; k < 3; ++k) wait_flags(ctl + C_FW + k * 64 + lane, k * 64 + lane < 144, E);
      const rsrc_t rF = mkrs(ws + OFF_WF + (size_t)dn * 72 * 4608, 72 * 4608);
#pragma unroll 4
      for (int p = 0; p < JL * 18; ++p) dma16c(rF, smem + L_WPF + p * 1024, v16, p * 1024);
      { const char* fv = ws + OFF_WF + (size_t)dn * 72 * 4608 + JL * 4 * 4608 + (size_t)(bl & 3) * 73728 + lane * 128;
#pragma unroll
        for (int k = 0; k < 9; ++k) touchc(fv + k * 8192); }
      WAITV(9);
    } else {
      wait_flags(ctl + C_F3 + (t3 >> 1) * 64 + lane, true, E);
      STAMP(2);
      { const char* hv = ws + OFF_H + (size_t)t3 * 4 * 73728 + (size_t)dn * 36864 + lane * 128;
#pragma unroll
        for (int k = 0; k < 4; ++k) touchc(hv + k * 8192);
        if (lane < 32) touchc(hv + 4 * 8192); }
    }
    BAR();
    if (lw == 0) {
      for (unsigned sp = 0; sp < (SPIN_CAP << 6) && *(volatile LAS unsigned*)(smem + L_PROG) == 0u; ++sp) __builtin_amdgcn_s_sleep(4);
      wait_flags<8>(ctl + C_FA + dn * 64 + lane, lane < 18, E);
      const rsrc_t rA = mkrs(ws + OFF_AT + (size_t)dn * 72 * 4096, 72 * 4096);
#pragma unroll
      for (int p = 0; p < 9; ++p) dma16c(rA, smem + L_AT + p * 1024, (lane >> 3) * 4096 + (lane & 7) * 16, p * 8 * 4096 + t3 * 128);
      const rsrc_t rG = mkrs(ws + OFF_GATE + t3 * 256, 256);
      dma16c(rG, smem + L_AT + 9216, v16, 0);
    } else if (ssm) {
      const rsrc_t rA = mkrs(ws + OFF_AT + (size_t)(36 * tn + 4 * tm) * 4096, 4 * 4096);
#pragma unroll 4
      for (int p = 0; p < 16; ++p) __builtin_amdgcn_raw_buffer_store_b128(*(LAS u32x4*)(smem + (p >> 2) * CV_W + (p & 3) * 1024 + v16), rA, v16, p * 1024, 16);
      WAITV(0);
      if (lane == 0) __hip_atomic_store((GAS unsigned*)ctl + C_FA + (tn >> 1) * 64 + (tn & 1) * 9 + tm, E, RLX);
    }
    WAITV(0);
    BAR();
    BAR();
    return;
  }

  relane();
  const int dch = 36 * tn + 4 * tm + wid;
  const int dsf = ssm ? dch : 0;
  const float dskf = __uint_as_float((((const unsigned*)dsk)[dsf >> 1] >> (16 * (dsf & 1))) << 16);
  const int rh = wid >> 1, ch = wid & 1;
  int xo[2][2], wo[5];
#pragma unroll
  for (int kk = 0; kk < 2; ++kk)
#pragma unroll
    for (int h = 0; h < 2; ++h) xo[kk][h] = (64 * rh + 32 * (h ^ ch) + fr) * 128 + (((4 * kk + fq) ^ ((fr >> 1) & 7)) << 4);
  { const int q4 = fr >> 2, p4 = lane & 3, cb = ((288 * tn) & 127) >> 4, sz = (q4 & 2) | ((fq & 1) << 2);
#pragma unroll
    for (int b = 0; b < 5; ++b) wo[b] = 16384 + (8 * fq + q4) * 384 + (((cb + 2 * (b & 3) + (p4 >> 1)) ^ sz) << 4) + 8 * (p4 & 1) + (b < 4 ? 128 * ch : 256); }
  auto rdz = [&](ZF& F, int sl, int kk) {
    LAS char* s = smem + sl;
#pragma unroll
    for (int i = 0; i < 4; ++i) F.x[i] = *(LAS bf16x8*)(s + xo[kk][i >> 1] + (i & 1) * 2048);
#pragma unroll
    for (int b = 0; b < 5; ++b)
#pragma unroll
      for (int t = 0; t < 2; ++t) F.w[b][t] = __builtin_amdgcn_ds_read_tr16_b64_v4i16((LAS s16x4*)(s + wo[b] + (32 * kk + 4 * t) * 384));
  };
  f32x4 za[4][4], z8[2];
  auto zzero = [&]() {
#pragma unroll
    for (int i = 0; i < 4; ++i)
#pragma unroll
      for (int b = 0; b < 4; ++b) za[i][b] = (f32x4){0, 0, 0, 0};
    z8[0] = (f32x4){0, 0, 0, 0}; z8[1] = (f32x4){0, 0, 0, 0}; };
  auto mmz = [&](const ZF& F, bool z2) {
#pragma unroll
    for (int b = 0; b < 5; ++b) { const bf16x8 wv = __builtin_shufflevector(F.w[b][0], F.w[b][1], 0, 1, 2, 3, 4, 5, 6, 7);
#pragma unroll
      for (int i = 0; i < (b < 4 ? 4 : 2); ++i) { f32x4& a = b < 4 ? za[i][b & 3] : z8[i & 1];
        a = z2 ? __builtin_amdgcn_mfma_f32_16x16x32_bf16(F.x[i], wv, a, 0, 0, 0) : __builtin_amdgcn_mfma_f32_16x16x32_bf16(wv, F.x[i], a, 0, 0, 0); } }
#pragma unroll
    for (int i = 0; i < 14; ++i) { __builtin_amdgcn_sched_group_barrier(0x8, 1, 0); __builtin_amdgcn_sched_group_barrier(0x100, 1, 0); }
    __builtin_amdgcn_sched_group_barrier(0x8, 4, 0); SB0();
  };
  ZF F0, F1;
  int sl = 0;
  auto zh1 = [&](bool z2) { rdz(F1, sl, 1); mmz(F0, z2); };
  auto zbar = [&]() { WAITL0(); BAR(); sl = (sl == 2 * ZST) ? 0 : sl + ZST; };
  auto zh2 = [&](bool z2) { rdz(F0, sl, 0); mmz(F1, z2); };
  zzero();
  const rsrc_t rXT = mkrs(ws + OFF_XT + (size_t)(tn * 64 + tm * 4 + wid) * 2304, 2304);
  STAMP(1);
  BAR();
  STAMP(2);
  rdz(F0, 0, 0);
  if (ssm) {
    LAS char* kp = smem + L_KG + (TMODE == 2 ? (4 + wid) * 4352 : wid * 3072) + v16;
    LAS u16* kc = (LAS u16*)(smem + L_KC + wid * 1344);
#pragma unroll
    for (int t = 0; t < 3; ++t) kd[t] = (kd[t] + *(LAS f32x4*)(kp + t * 1024)) * ksc;
#pragma unroll
    for (int j = 0; j < 4; ++j) { const int r = fq * 4 + j;
      kc[128 * (r >> 3) + (r & 7) + 8 * fr] = (u16)pk2(kd[0][j], 0.f);
      kc[r < 8 ? 256 + 64 * (r >> 2) + (r & 3) + 4 * fr : 384 + 32 * ((r - 8) >> 1) + (r & 1) + 2 * fr] = (u16)pk2(kd[1][j], 0.f);
      if (r < 8 || (r == 8 && fr == 0)) kc[512 + 16 * r + fr] = (u16)pk2(kd[2][j], 0.f); }
  }
#pragma unroll 1
  for (int g = 0; g < 9; ++g) {
    zh1(true);
    if (g == (9 * tn) >> 4 || g == (9 * tn + 8) >> 4) {
      const int q4 = fr >> 2, p4 = lane & 3;
#pragma unroll
      for (int ct = 0; ct < 3; ++ct) {
        const int lq = (9 * tn + 4 * ct + p4) & 15; s16x4 h[2];
#pragma unroll
        for (int e = 0; e < 2; ++e) { const int rho = 32 * wid + 16 * e + 4 * fq + q4;
          h[e] = __builtin_amdgcn_ds_read_tr16_b64_v4i16((LAS s16x4*)(smem + sl + rho * 128 + (((lq >> 1) ^ ((rho >> 1) & 7)) << 4) + 8 * (lq & 1))); }
        const int myq = 4 * ct + (fr >> 2);
        if (myq < 9 && ((9 * tn + myq) >> 4) == g)
          __builtin_amdgcn_raw_buffer_store_b128(__builtin_bit_cast(u32x4, __builtin_shufflevector(h[0], h[1], 0, 1, 2, 3, 4, 5, 6, 7)), rXT, (16 * ct + fr) * 64 + fq * 16, 0, 16);
      }
    }
    zbar(); zh2(true);
  }
  STAMP(3);
  u32x4 zr[2][4], zr8;
#pragma unroll
  for (int P = 0; P < 2; ++P) {
    f32x2 S[4], Q[4];
#pragma unroll
    for (int k = 0; k < 4; ++k) { S[k] = (f32x2){0, 0}; Q[k] = (f32x2){0, 0}; }
#pragma unroll
    for (int b = 0; b < (P ? 4 : 5); ++b)
#pragma unroll
      for (int k = 0; k < 4; ++k) { const f32x4 c = b < 4 ? za[2 * P + (k >> 1)][b & 3] : z8[k >> 1]; f32x2 v = (k & 1) ? (f32x2){c[2], c[3]} : (f32x2){c[0], c[1]};
        v = gelu2(v); S[k] += v; Q[k] += v * v; if (b < 4) zr[P][b & 3][k] = pk2(v.x, v.y); else zr8[k] = pk2(v.x, v.y); }
#pragma unroll
    for (int k = 0; k < 4; ++k) { S[k].x = rowsum16(S[k].x); S[k].y = rowsum16(S[k].y); Q[k].x = rowsum16(Q[k].x); Q[k].y = rowsum16(Q[k].y); }
    if (fr == 0) {
      LAS f32x4* d = (LAS f32x4*)(smem + L_PS + ch * 1024 + (64 * rh + 32 * (P ^ ch) + 8 * fq) * 4);
      d[0] = (f32x4){S[0].x, S[0].y, S[1].x, S[1].y}; d[1] = (f32x4){S[2].x, S[2].y, S[3].x, S[3].y};
      d[32] = (f32x4){Q[0].x, Q[0].y, Q[1].x, Q[1].y}; d[33] = (f32x4){Q[2].x, Q[2].y, Q[3].x, Q[3].y};
    }
  }
#pragma unroll
  for (int b = 0; b < 4; ++b) asm volatile("" : "+v"(zr[0][b]), "+v"(zr[1][b]));
  asm volatile("" : "+v"(zr8));
  STAMP(4);
  zzero();
  zh1(false); zbar();
  const unsigned E = __builtin_amdgcn_readfirstlane(*(volatile LAS unsigned*)(smem + L_EPO));
  {
    const int row = 32 * wid + 8 * fq + (fr & 7);
    LAS float* ps = (LAS float*)(smem + L_PS) + (fr >> 3) * 128 + row;
    GAS unsigned long long* gp = (GAS unsigned long long*)(ws + OFF_ST + ((tm * 16 + tn) * 128 + row) * 16 + (fr >> 3) * 8);
    __hip_atomic_store(gp, ((unsigned long long)E << 32) | __float_as_uint(ps[0] + ps[256]), RLX);
  }
  zh2(false);
#pragma unroll 1
  for (int g = 1; g < HS; ++g) { zh1(false); zbar(); zh2(false); }
  {
    const rsrc_t rS = mkrs(ws + OFF_ST + (size_t)tm * 16 * 2048, 16 * 2048);
    u32x4 gr[8];
    auto ldgr = [&]() {
#pragma unroll
      for (int k = 0; k < 8; ++k) gr[k] = __builtin_amdgcn_raw_buffer_load_b128(rS, ((lane >> 5) * 8 * 128 + 32 * wid + (lane & 31)) * 16, k * 2048, 16); };
    ldgr(); SB0();
    zh1(false); zbar(); zh2(false); zh1(false);
#pragma unroll
    for (int k = 0; k < 8; ++k) asm volatile("" : "+v"(gr[k]));
    float s, q;
    for (unsigned sp = 0;; ++sp) {
      bool ok = true; s = 0.f; q = 0.f;
#pragma unroll
      for (int k = 0; k < 8; ++k) { ok = ok && gr[k][1] == E && gr[k][3] == E; s += __uint_as_float(gr[k][0]); q += __uint_as_float(gr[k][2]); }
      if (__all(ok) || sp >= SPIN_CAP) break;
      __builtin_amdgcn_s_sleep(1);
      ldgr();
    }
    s += __shfl_xor(s, 32); q += __shfl_xor(q, 32);
    const float mean = s * (1.f / 2304.f), var = fmaxf(q * (1.f / 2304.f) - mean * mean, 0.f), rs = __builtin_amdgcn_rsqf(var + 1e-5f);
    if (lane < 32) *(LAS f32x2*)(smem + L_RN + (32 * wid + lane) * 8) = (f32x2){rs, -mean * rs};
    STAMP(22);
    zbar();
    const rsrc_t rZs = mkrs(ws + OFF_Z2, SZ_F);
#pragma unroll
    for (int P = 0; P < 2; ++P) {
      f32x4 rn[4];
#pragma unroll
      for (int k = 0; k < 4; ++k) rn[k] = *(LAS f32x4*)(smem + L_RN + (64 * rh + 32 * (P ^ ch) + 8 * fq) * 8 + k * 16);
      const int zo = (((2 * tm + rh) * 144 + 9 * tn) * 2 + (P ^ ch)) * 1024;
#pragma unroll
      for (int b = 0; b < (P ? 4 : 5); ++b) { const u32x4 z = b < 4 ? zr[P][b & 3] : zr8; u32x4 o;
#pragma unroll
        for (int k = 0; k < 4; ++k) o[k] = pk2(bflo(z[k]) * rn[k][0] + rn[k][1], bfhi(z[k]) * rn[k][2] + rn[k][3]);
        __builtin_amdgcn_raw_buffer_store_b128(o, rZs, v16, zo + (b < 4 ? 4 * ch + b : 8) * 2048, 16); }
    }
    SB0();
    STAMP(5);
    zh2(false);
  }
#pragma unroll 1
  for (int g = HS + 2; g < 9; ++g) {
    zh1(false);
    if (g == FS) { WAITV(0); if (lane == 0) __hip_atomic_store((GAS unsigned*)ctl + C_F2 + tn * 64 + tm * 4 + wid, E, RLX); }
    zbar(); zh2(false);
  }
  STAMP(6);
  {
    LAS char* pk = smem + L_PARK;
#pragma unroll
    for (int i = 0; i < 4; ++i) { const int m = 4 * rh + ((i + 2 * ch) & 3);
#pragma unroll
      for (int c = 0; c < 2; ++c) { u32x4 o;
#pragma unroll
        for (int k = 0; k < 4; ++k) { const f32x4 cc = za[i][2 * c + (k >> 1)]; f32x2 v = (k & 1) ? (f32x2){cc[2], cc[3]} : (f32x2){cc[0], cc[1]}; v = gelu2(v); o[k] = pk2(v.x, v.y); }
        *(LAS u32x4*)(pk + (m * 4 + 2 * ch + c) * 1024 + v16) = o; }
      if (i < 2) { const f32x2 v0 = gelu2((f32x2){z8[i][0], z8[i][1]}), v1 = gelu2((f32x2){z8[i][2], z8[i][3]});
        *(LAS u32x2*)(pk + 32768 + m * 512 + lane * 8) = (u32x2){pk2(v0.x, v0.y), pk2(v1.x, v1.y)}; }
    }
  }
  float bb[2];
  { LAS u16* bs = (LAS u16*)(smem + L_BIAS) + 32 * wid + fr; bb[0] = bf2f(bs[0]); bb[1] = bf2f(bs[16]); }
  STAMP(7);
  const int kh = wid & 1, th = wid >> 1;
  u32x4 ul[4];
  f32x4 acc[9][4];
  {
    const int zoff = 16384 + kh * 1024 + v16;
    const int woff = (th * 64 + fr) * 128 + (((kh * 4 + fq) ^ ((fr >> 1) & 7)) << 4);
    const int woA = woff + kh * 4096, woB = woff + 4096 - kh * 4096;
#pragma unroll
    for (int a = 0; a < 9; ++a)
#pragma unroll
      for (int b = 0; b < 4; ++b) { const float iv = b < 2 ? bb[b] : 0.f; acc[a][b] = (f32x4){iv, iv, iv, iv}; }
    bf16x8 Z0[9], W0[4], Z1[9], W1[4];
    auto rd = [&](bf16x8(&Z)[9], bf16x8(&W)[4], int sl) {
      LAS char* s = smem + sl;
#pragma unroll
      for (int b = 0; b < 4; ++b) W[b] = *(LAS bf16x8*)(s + (b < 2 ? woA : woB) + (b & 1) * 2048);
#pragma unroll
      for (int a = 0; a < 9; ++a) Z[a] = *(LAS bf16x8*)(s + zoff + a * 2048);
    };
    auto mm = [&](bf16x8(&Z)[9], bf16x8(&W)[4]) {
#pragma unroll
      for (int a = 0; a < 9; ++a)
#pragma unroll
        for (int b = 0; b < 4; ++b) acc[a][b] = __builtin_amdgcn_mfma_f32_16x16x32_bf16(Z[a], W[b], acc[a][b], 0, 0, 0);
    };
    auto sched = [&]() {
#pragma unroll
      for (int i = 0; i < 13; ++i) { __builtin_amdgcn_sched_group_barrier(0x8, 2, 0); __builtin_amdgcn_sched_group_barrier(0x100, 1, 0); }
      __builtin_amdgcn_sched_group_barrier(0x8, 10, 0); SB0();
    };
    WAITL0(); BAR();
    STAMP(8);
    rd(Z0, W0, 0);
    sl = 0;
#pragma unroll 1
    for (int i = 0; i < 32; i += 2) {
      sl = (sl == 2 * GST) ? 0 : sl + GST;
      WAITL0(); BAR();
      rd(Z1, W1, sl); mm(Z0, W0); sched();
      sl = (sl == 2 * GST) ? 0 : sl + GST;
      WAITL0(); BAR();
      rd(Z0, W0, sl); mm(Z1, W1); sched();
    }
    relane();
    STAMP(9);
    WAITL0(); BAR();
    LAS char* dst = smem + wid * 18432 + v16;
#pragma unroll
    for (int a = 0; a < 9; ++a)
#pragma unroll
      for (int u = 0; u < 2; ++u) *(LAS f32x4*)(dst + (a * 2 + u) * 1024) = acc[a][2 + u];
  }
  WAITL0(); BAR();
  STAMP(20);
  {
    const rsrc_t rHs = mkrs(ws + OFF_H, SZ_F);
    LAS char* src = smem + (wid ^ 1) * 18432 + v16;
    LAS char* pk = smem + L_PARK + wid * 8192 + v16;
    const u32x2 y8[2] = {*(LAS u32x2*)(smem + L_PARK + 32768 + wid * 1024 + lane * 8), *(LAS u32x2*)(smem + L_PARK + 32768 + wid * 1024 + 512 + lane * 8)};
#pragma unroll
    for (int u = 0; u < 2; ++u) {
      f32x4 g[9];
#pragma unroll
      for (int a = 0; a < 9; ++a) g[a] = acc[a][u] + *(LAS f32x4*)(src + (a * 2 + u) * 1024);
      const int tb = tm * 8 + 2 * wid + u, ho = (tb * 72 + 9 * pp + 4 * ee) * 1024;
#pragma unroll
      for (int c = 0; c < 4; ++c) { const u32x4 z = *(LAS u32x4*)(pk + (u * 4 + c) * 1024); u32x4 o;
        o[0] = pk2(bflo(z[0]) * g[2 * c][0], bfhi(z[0]) * g[2 * c][1]); o[1] = pk2(bflo(z[1]) * g[2 * c][2], bfhi(z[1]) * g[2 * c][3]);
        o[2] = pk2(bflo(z[2]) * g[2 * c + 1][0], bfhi(z[2]) * g[2 * c + 1][1]); o[3] = pk2(bflo(z[3]) * g[2 * c + 1][2], bfhi(z[3]) * g[2 * c + 1][3]);
        __builtin_amdgcn_raw_buffer_store_b128(o, rHs, v16, ho + c * 1024, 16); }
      u32x2 o; o[0] = pk2(bflo(y8[u][0]) * g[8][0], bfhi(y8[u][0]) * g[8][1]); o[1] = pk2(bflo(y8[u][1]) * g[8][2], bfhi(y8[u][1]) * g[8][3]);
      __builtin_amdgcn_raw_buffer_store_b64(o, rHs, ee * 512 + (fr + 16 * (fq >> 1)) * 16 + (fq & 1) * 8, (tb * 72 + 9 * pp + 8) * 1024, 16);
    }
  }
#pragma unroll
  for (int k = 0; k < 4; ++k) ul[k] = *(LAS u32x4*)(smem + L_US + wid * 4096 + lane * 64 + k * 16);
  WAITL0(); BAR();
  STAMP(10);
  if (ssm) {
    LAS char* cv = smem + wid * CV_W;
    LAS char* ub = cv + CV_U;
    {
#pragma unroll
      for (int k = 0; k < 4; ++k) { const u32x4 s = ul[3 - k]; u32x4 o;
#pragma unroll
        for (int m = 0; m < 4; ++m) o[m] = __builtin_amdgcn_alignbit(s[3 - m], s[3 - m], 16);
        *(LAS u32x4*)(ub + (15 + lane) * 96 + k * 16) = o;
        if (lane < 15) *(LAS u32x4*)(ub + lane * 96 + k * 16) = (u32x4){0, 0, 0, 0}; }
    }
    const int cb0 = 248, cb1 = 4608 + 64, cb2 = 2 * 4608 + 128, cb3 = 3 * 4608 + 192;
    {
      LAS u16* kc = (LAS u16*)(smem + L_KC + wid * 1344);
      auto emit = [&](int k, const unsigned (&pr)[6]) { const int o = 64 + 512 * k + 8 * lane;
        *(LAS u32x2*)(cv + cb0 + o) = (u32x2){pr[0], pr[2]}; *(LAS u32x2*)(cv + cb1 + o) = (u32x2){pr[1], pr[3]};
        *(LAS u32x2*)(cv + cb2 + o) = (u32x2){pr[2], pr[4]}; *(LAS u32x2*)(cv + cb3 + o) = (u32x2){pr[3], pr[5]}; };
      LAS char* kb = (LAS char*)kc;
      const u32x2 q0 = *(LAS u32x2*)(kb + 8 * lane), q1 = *(LAS u32x2*)(kb + 8 * lane + 8);
      const unsigned r0 = *(LAS unsigned*)(kb + 512 + 4 * lane), r1 = *(LAS unsigned*)(kb + 516 + 4 * lane);
      unsigned ab[6], cx[2];
#pragma unroll
      for (int k = 2; k < 4; ++k) { const int m = 256 + 64 * k + lane; ab[k - 2] = kc[m] | ((unsigned)kc[m + 1] << 16); cx[k - 2] = kc[m + 2]; }
#pragma unroll
      for (int k = 4; k < 8; ++k) { const int m = 384 + 32 * k + (lane >> 1); ab[k - 2] = kc[m] | ((unsigned)kc[m + 1] << 16); }
      const unsigned k0 = kc[0], k1 = kc[1], k2 = kc[2];
      const bool l63 = lane == 63;
      { const unsigned p45 = l63 ? __builtin_amdgcn_perm(q1[0], q1[0], 0x01000100u) : q1[0], p56 = l63 ? q1[0] : __builtin_amdgcn_alignbit(q1[1], q1[0], 16);
        const unsigned pr[6] = {q0[0], __builtin_amdgcn_alignbit(q0[1], q0[0], 16), q0[1], __builtin_amdgcn_alignbit(q1[0], q0[1], 16), p45, p56}; emit(0, pr); }
      { const unsigned v22 = __builtin_amdgcn_perm(r1, r1, 0x01000100u);
        const unsigned pr[6] = {__builtin_amdgcn_perm(r0, r0, 0x01000100u), r0, __builtin_amdgcn_alignbit(r1, r0, 16), v22, v22, l63 ? v22 : r1}; emit(1, pr); }
      { const bool od = lane & 1; const unsigned s1 = od ? 0x03020100u : 0x01000100u, s2 = od ? 0x03020302u : 0x03020100u, s5 = od ? 0x03020302u : 0x05040302u;
#pragma unroll
        for (int k = 2; k < 4; ++k) { const unsigned w = ab[k - 2], bb2 = __builtin_amdgcn_perm(w, w, 0x03020302u);
          const unsigned pr[6] = {__builtin_amdgcn_perm(w, w, 0x01000100u), __builtin_amdgcn_perm(w, w, s1), __builtin_amdgcn_perm(w, w, s2), bb2, bb2, __builtin_amdgcn_perm(cx[k - 2], w, s5)}; emit(k, pr); } }
      { const int na = (0x0415 >> (4 * (lane & 3))) & 15; unsigned s3[5];
#pragma unroll
        for (int n = 0; n < 5; ++n) s3[n] = (n < na ? 0x0100u : 0x0302u) | (n + 1 < na ? 0x01000000u : 0x03020000u);
#pragma unroll
        for (int k = 4; k < 8; ++k) { const unsigned w = ab[k - 2];
          const unsigned pr[6] = {__builtin_amdgcn_perm(w, w, s3[0]), __builtin_amdgcn_perm(w, w, s3[1]), __builtin_amdgcn_perm(w, w, s3[2]), __builtin_amdgcn_perm(w, w, s3[3]),
                                  __builtin_amdgcn_perm(w, w, s3[4]), __builtin_amdgcn_perm(w, w, 0x03020302u)}; emit(k, pr); } }
      if (lane < 8) { const bool l7 = lane == 7;
        *(LAS u32x2*)(cv + cb0 + 8 * lane) = (u32x2){0u, 0u};
        *(LAS u32x2*)(cv + cb1 + 8 * lane) = (u32x2){0u, l7 ? (k0 << 16) : 0u};
        *(LAS u32x2*)(cv + cb2 + 8 * lane) = (u32x2){0u, l7 ? (k0 | (k1 << 16)) : 0u};
        *(LAS u32x2*)(cv + cb3 + 8 * lane) = (u32x2){l7 ? (k0 << 16) : 0u, l7 ? (k1 | (k2 << 16)) : 0u}; }
    }
    WAITV(0);
    if (lane == 0) __hip_atomic_store((GAS unsigned*)ctl + C_F3 + tm * 64 + tn * 4 + wid, E, RLX);
    STAMP(11);
    STAMP(18);
    const int sg = (fr + 1) & 3;
    LAS char* pA = cv + (sg == 0 ? cb0 : sg == 1 ? cb1 : sg == 2 ? cb2 : cb3) + 2 * (8 * (fr >> 2) + 8 * fq + (((fr & 3) + 1) & 4));
    LAS char* pB = cv + CV_U + 96 * (15 + fr) + 16 * fq - 96 * 63;
    f32x4 ya[4][2];
#pragma unroll
    for (int c = 0; c < 4; ++c) { ya[c][0] = (f32x4){0, 0, 0, 0}; ya[c][1] = (f32x4){0, 0, 0, 0}; }
    {
      u32x4 ra[2][4][2]; bf16x8 rb[2][4];
      pB += 96 * 62;
      auto rdc = [&](int s, int kk) {
#pragma unroll
        for (int e = 0; e < 4; ++e) {
          rb[s][e] = *(LAS bf16x8*)(pB + 96 * (1 - kk) + 1536 * e);
          const u32x4 lo = *(LAS u32x4*)(pA + 64 * kk + 1024 * e); const u32x2 hi = *(LAS u32x2*)(pA + 64 * kk + 1024 * e + 16);
          ra[s][e][0] = lo; ra[s][e][1] = (u32x4){lo[2], lo[3], hi[0], hi[1]}; }
      };
      auto mmc = [&](int s) {
#pragma unroll
        for (int e = 0; e < 4; ++e)
#pragma unroll
          for (int c = e; c < 4; ++c) {
            ya[c][0] = __builtin_amdgcn_mfma_f32_16x16x32_bf16(__builtin_bit_cast(bf16x8, ra[s][e][0]), rb[s][c - e], ya[c][0], 0, 0, 0);
            ya[c][1] = __builtin_amdgcn_mfma_f32_16x16x32_bf16(__builtin_bit_cast(bf16x8, ra[s][e][1]), rb[s][c - e], ya[c][1], 0, 0, 0); }
#pragma unroll
        for (int i = 0; i < 12; ++i) { __builtin_amdgcn_sched_group_barrier(0x8, 1, 0); __builtin_amdgcn_sched_group_barrier(0x100, 1, 0); }
        __builtin_amdgcn_sched_group_barrier(0x8, 8, 0);
        SB0();
      };
      rdc(0, 0);
      SB0();
#pragma unroll 1
      for (int g = 0; g < 8; ++g) {
        rdc(1, 1); mmc(0);
        pA += 128; pB -= 192;
        rdc(0, 0); mmc(1);
      }
    }
    STAMP(19);
#pragma unroll
    for (int c = 0; c < 4; ++c) { const u32x4 uu = *(LAS u32x4*)(ub + 96 * (15 + 16 * c + fr) + 16 * (3 - fq)); u32x4 o;
      o[0] = pk2(ya[c][0][0] + bfhi(uu[3]) * dskf, ya[c][0][1] + bflo(uu[3]) * dskf); o[1] = pk2(ya[c][0][2] + bfhi(uu[2]) * dskf, ya[c][0][3] + bflo(uu[2]) * dskf);
      o[2] = pk2(ya[c][1][0] + bfhi(uu[1]) * dskf, ya[c][1][1] + bflo(uu[1]) * dskf); o[3] = pk2(ya[c][1][2] + bfhi(uu[0]) * dskf, ya[c][1][3] + bflo(uu[0]) * dskf);
      *(LAS u32x4*)(cv + c * 1024 + fr * 64 + fq * 16) = o; }
  } else {
    WAITV(0);
    if (lane == 0) __hip_atomic_store((GAS unsigned*)ctl + C_F3 + tm * 64 + tn * 4 + wid, E, RLX);
    STAMP(11);
  }
  STAMP(12);
  relane();
  constexpr int DEPTH = 3;
  {
    const rsrc_t rH = mkrs(ws + OFF_H + (size_t)t3 * 4 * 72 * 1024, 4 * 72 * 1024);
    const rsrc_t rF = mkrs(ws + OFF_WF + (size_t)dn * 72 * 4608, 72 * 4608);
    const int vh = 4096 + (fq * 8 + (fr & 7)) * 16;
    bf16x8 H[DEPTH][4], W[DEPTH][5];
    auto ldH = [&](int s, int j) { const int P = j * 4 + wid;
#pragma unroll
      for (int b = 0; b < 4; ++b) H[s][b] = __builtin_bit_cast(bf16x8, __builtin_amdgcn_raw_buffer_load_b128(rH, v16, P * 1024 + b * 72 * 1024, AUXH)); };
    auto ldW = [&](int s, int j) { const int P = j * 4 + wid;
      if (j < JL) { LAS char* p = smem + L_WPF + P * 4608;
#pragma unroll
        for (int c = 0; c < 4; ++c) W[s][c] = *(LAS bf16x8*)(p + v16 + c * 1024);
        W[s][4] = *(LAS bf16x8*)(p + vh);
      } else {
#pragma unroll
        for (int c = 0; c < 4; ++c) W[s][c] = __builtin_bit_cast(bf16x8, __builtin_amdgcn_raw_buffer_load_b128(rF, v16 + c * 1024, P * 4608, AUXW));
        W[s][4] = __builtin_bit_cast(bf16x8, __builtin_amdgcn_raw_buffer_load_b128(rF, vh, P * 4608, AUXW));
      } };
    WAITL0(); BAR();
    STAMP(13);
#pragma unroll
    for (int s = 0; s < DEPTH; ++s) { ldH(s, s); ldW(s, s); }
    SB0();
    f32x4 ac3[4][5];
#pragma unroll
    for (int b = 0; b < 4; ++b)
#pragma unroll
      for (int c = 0; c < 5; ++c) ac3[b][c] = (f32x4){0, 0, 0, 0};
#pragma unroll
    for (int j = 0; j < 18; ++j) {
      const int s = j % DEPTH;
#pragma unroll
      for (int b = 0; b < 4; ++b)
#pragma unroll
        for (int c = 0; c < 5; ++c) ac3[b][c] = __builtin_amdgcn_mfma_f32_16x16x32_bf16(H[s][b], W[s][c], ac3[b][c], 0, 0, 0);
      if (j == PROGJ && wid == 0 && lane == 0) *(volatile LAS unsigned*)(smem + L_PROG) = 1u;
      if (j + DEPTH < 18) { ldH(s, j + DEPTH); ldW(s, j + DEPTH); }
      SB0();
    }
    STAMP(14);
    WAITL0(); BAR();
    f32x4 mine[5];
#pragma unroll
    for (int b = 0; b < 4; ++b) {
      if (b == wid) {
#pragma unroll
        for (int c = 0; c < 5; ++c) mine[c] = ac3[b][c];
      } else {
        LAS char* dst = smem + (b * 3 + ((wid - b - 1) & 3)) * 5120 + v16;
#pragma unroll
        for (int c = 0; c < 5; ++c) *(LAS f32x4*)(dst + c * 1024) = ac3[b][c];
      }
    }
    WAITL0(); BAR();
    STAMP(21);
    LAS char* src = smem + wid * 3 * 5120 + v16;
    const f32x4 gv = *(LAS f32x4*)(smem + L_AT + 9216 + (wid * 16 + fq * 4) * 4);
    const int t0 = t3 * 64 + wid * 16 + fq * 4;
#pragma unroll
    for (int c = 0; c < 5; ++c) {
      const f32x4 v = mine[c] + *(LAS f32x4*)(src + c * 1024) + (*(LAS f32x4*)(src + 5120 + c * 1024) + *(LAS f32x4*)(src + 10240 + c * 1024));
      const u32x2 av = *(LAS u32x2*)(smem + L_AT + (c * 16 + (c < 4 ? fr : (fr & 7))) * 128 + (wid * 16 + fq * 4) * 2);
      const unsigned p0 = pk2(gv[0] * bflo(av[0]) + (1.f - gv[0]) * v[0], gv[1] * bfhi(av[0]) + (1.f - gv[1]) * v[1]);
      const unsigned p1 = pk2(gv[2] * bflo(av[1]) + (1.f - gv[2]) * v[2], gv[3] * bfhi(av[1]) + (1.f - gv[3]) * v[3]);
      if (c < 4 || fr < 8) { u16* o = out + (size_t)t0 * 576 + dn * 72 + c * 16 + fr;
        o[0] = (u16)p0; o[576] = (u16)(p0 >> 16); o[1152] = (u16)p1; o[1728] = (u16)(p1 >> 16); }
    }
  }
  STAMP(15);
}

extern "C" void kernel_launch(void* const* d_in, const int* in_sizes, int n_in, void* d_out, int out_size, void* d_ws, size_t ws_size, hipStream_t stream) {
  (void)in_sizes; (void)n_in; (void)out_size; (void)ws_size;
  (void)hipFuncSetAttribute((const void*)gmix_fused, hipFuncAttributeMaxDynamicSharedMemorySize, LDS_BYTES);
  gmix_fused<<<256, 512, LDS_BYTES, stream>>>((const u16*)d_in[0], (const u16*)d_in[1], (const u16*)d_in[2], (const u16*)d_in[3], (const u16*)d_in[4], (const u16*)d_in[5],
      (const u16*)d_in[6], (const u16*)d_in[8], (const u16*)d_in[9], (const u16*)d_in[10], (const u16*)d_in[11], (const u16*)d_in[12], (u16*)d_out, (char*)d_ws
  );
}
```

```cpp
#include <hip/hip_runtime.h>
#include <hip/hip_bf16.h>

#define LAS __attribute__((address_space(3)))
#define GAS __attribute__((address_space(1)))
typedef unsigned short u16;
typedef unsigned u32x4 __attribute__((ext_vector_type(4)));
typedef unsigned u32x2 __attribute__((ext_vector_type(2)));
typedef short bf16x8 __attribute__((ext_vector_type(8)));
typedef short s16x4 __attribute__((ext_vector_type(4)));
typedef _Float16 f16x8 __attribute__((ext_vector_type(8)));
typedef _Float16 f16x2 __attribute__((ext_vector_type(2)));
typedef __bf16 bfx2 __attribute__((ext_vector_type(2)));
typedef float f32x4 __attribute__((ext_vector_type(4)));
typedef float f32x2 __attribute__((ext_vector_type(2)));

constexpr int SZ_F = 9437184;
constexpr int OFF_Z2 = 0, OFF_H = SZ_F, OFF_WF = 2 * SZ_F, OFF_XT = OFF_WF + 2654208, OFF_AT = OFF_XT + 2359296,
              OFF_GATE = OFF_AT + 2359296, OFF_ST = OFF_GATE + 8192, OFF_CTL = OFF_ST + 524288, WS_END = OFF_CTL + 32768;
constexpr int C_R = 0, C_S = 256, C_A = 768, C_F2 = 1024, C_F3 = 2048, C_FW = 3072, C_FA = 3584;
constexpr int JLV = 2, HPRIO = 2, LPRIO = 3, F3SLP = 16, WTN = 10, HFG = 2, CTOUCH = 1, HSLP = 6, OAUX = 0, GSLP = 6, GP1 = 5, AUXZ = 16, AUXS = 0, ROT = 1, NT0 = 2, WSLP = 64, RNG = 13, FS = 6, AUXH = 16, AUXW = 16;
constexpr int ZST = 40960, GST = 34816, L_PARK = 104448, L_KG = 139264, L_PS = 122880, L_RN = 124928, L_RT = 125952, L_KC = 157696, L_AT = 146688, L_US = 0,
              L_KX = 141312, L_PROG = 163072, L_EPO = 163076, L_HC = 163080, L_BIAS = 163328, L_DUMP = 163584, LDS_BYTES = 163840;
constexpr int KCP = 17408, UCP = 7680, JL = JLV, JR = 3;
constexpr int cb0 = 248, cb1 = 4352 + 64, cb2 = 2 * 4352 + 128, cb3 = 3 * 4352 + 192;
__device__ __forceinline__ constexpr int l_kcp(int c) { return 34816 + c * 17408; }
__device__ __forceinline__ constexpr int l_wpf(int j) { return 104448 + j * 18432; }
constexpr int CLN = 13;
constexpr unsigned SPIN_CAP = 1u << 15;

#define BAR() do { asm volatile("" ::: "memory"); __builtin_amdgcn_s_barrier(); asm volatile("" ::: "memory"); } while (0)
#define WAITV(n) asm volatile("s_waitcnt vmcnt(%0)" ::"i"(n) : "memory")
#define WAITL0() do { __builtin_amdgcn_s_waitcnt(0xC07F); asm volatile("" ::: "memory"); } while (0)
#define SB0() __builtin_amdgcn_sched_barrier(0)
#define RLX __ATOMIC_RELAXED, __HIP_MEMORY_SCOPE_AGENT
typedef __amdgpu_buffer_rsrc_t rsrc_t;
__device__ __forceinline__ rsrc_t mkrs(const void* p, int bytes) { return __builtin_amdgcn_make_buffer_rsrc((void*)p, 0, bytes, 0x00020000); }
__device__ __forceinline__ void dma16(rsrc_t r, LAS char* dst, int voff, int soff) { __builtin_amdgcn_raw_ptr_buffer_load_lds(r, (LAS void*)dst, 16, voff, soff, 0, 0); }
__device__ __forceinline__ void dma16c(rsrc_t r, LAS char* dst, int voff, int soff) { __builtin_amdgcn_raw_ptr_buffer_load_lds(r, (LAS void*)dst, 16, voff, soff, 0, 16); }
__device__ __forceinline__ void dma16_asm(u32x4 rs, unsigned dst, int voff, int soff) { unsigned keep;
  asm volatile("s_mov_b32 %0, m0\n\ts_mov_b32 m0, %1\n\ts_nop 0\n\tbuffer_load_dwordx4 %2, %3, %4 offen lds\n\ts_mov_b32 m0, %0" : "=&s"(keep) : "s"(dst), "v"(voff), "s"(rs), "s"(soff) : "memory"); }
__device__ __forceinline__ u32x4 mkrs4(const void* p, int bytes) { const unsigned long long a = (unsigned long long)p; return (u32x4){(unsigned)a, (unsigned)(a >> 32) & 0xffffu, (unsigned)bytes, 0x00020000u}; }
__device__ __forceinline__ void st128(u32x4 v, rsrc_t r, int off) { asm volatile("" : "+v"(off)); __builtin_amdgcn_raw_buffer_store_b128(v, r, off, 0, 16); }
__device__ __forceinline__ unsigned pk2(float lo, float hi) { f32x2 v = {lo, hi}; return __builtin_bit_cast(unsigned, __builtin_convertvector(v, bfx2)); }
__device__ __forceinline__ float dot2bf(unsigned a, unsigned b, float c) { return __builtin_amdgcn_fdot2_f32_bf16(__builtin_bit_cast(bfx2, a), __builtin_bit_cast(bfx2, b), c, false); }
__device__ __forceinline__ float bflo(unsigned v) { return __uint_as_float(v << 16); }
__device__ __forceinline__ float bfhi(unsigned v) { return __uint_as_float(v & 0xffff0000u); }
__device__ __forceinline__ float bf2f(u16 v) { return __uint_as_float(((unsigned)v) << 16); }
__device__ __forceinline__ float rbf(float f) { return bflo(pk2(f, f)); }
__device__ __forceinline__ f32x2 gelu2(f32x2 v) {
  const f32x2 p = (v * v) * (-0.10294324f) + (-2.3022082f);
  const f32x2 a = v * p;
  f32x2 d; d.x = __builtin_amdgcn_exp2f(a.x); d.y = __builtin_amdgcn_exp2f(a.y);
  d = d + 1.0f;
  f32x2 r; r.x = __builtin_amdgcn_rcpf(d.x); r.y = __builtin_amdgcn_rcpf(d.y);
  return v * r;
}
template <int CTRL> __device__ __forceinline__ float dppadd(float x) { return x + __builtin_amdgcn_update_dpp(0.f, x, CTRL, 0xf, 0xf, true); }
template <int CTRL> __device__ __forceinline__ float dppmax(float x) { return fmaxf(x, __builtin_amdgcn_update_dpp(0.f, x, CTRL, 0xf, 0xf, true)); }
__device__ __forceinline__ float rowsum16(float x) { x = dppadd<0x128>(x); x = dppadd<0x124>(x); x = dppadd<0x122>(x); x = dppadd<0x121>(x); return x; }
template <int SLP = 1>
__device__ __forceinline__ void wait_flags(const unsigned* f, bool act, unsigned E) {
  for (unsigned sp = 0; sp < SPIN_CAP; ++sp) {
    const unsigned v = __hip_atomic_load((GAS unsigned*)f, RLX);
    if (__all(!act || v == E)) break;
    __builtin_amdgcn_s_sleep(SLP);
  }
  asm volatile("" ::: "memory");
}
template <int AUX> __device__ __forceinline__ void touch_(LAS char* smem, const void* p) { __builtin_amdgcn_global_load_lds((const GAS unsigned*)p, (LAS unsigned*)(smem + L_DUMP), 4, 0, AUX); }
__device__ __forceinline__ void touch_asm(const void* p) { unsigned keep;
  asm volatile("s_mov_b32 %0, m0\n\ts_mov_b32 m0, %2\n\ts_nop 0\n\tglobal_load_lds_dword %1, off\n\ts_mov_b32 m0, %0" : "=&s"(keep) : "v"(p), "s"((unsigned)L_DUMP) : "memory"); }
__device__ __forceinline__ void touch_asm_c(const void* p) { unsigned keep;
  asm volatile("s_mov_b32 %0, m0\n\ts_mov_b32 m0, %2\n\ts_nop 0\n\tglobal_load_lds_dword %1, off sc1\n\ts_mov_b32 m0, %0" : "=&s"(keep) : "v"(p), "s"((unsigned)L_DUMP) : "memory"); }
#define touch(p) touch_<0>(smem, p)
#define touchc(p) touch_<16>(smem, p)

__device__ __forceinline__ float fmul_(float a, float b) { float r; asm("v_mul_f32 %0, %1, %2" : "=v"(r) : "v"(a), "v"(b)); return r; }
__device__ __forceinline__ float ffma_(float a, float b, float c) { float r; asm("v_fma_f32 %0, %1, %2, %3" : "=v"(r) : "v"(a), "v"(b), "v"(c)); return r; }
__device__ __forceinline__ float fnma_(float a, float b, float c) { float r; asm("v_fma_f32 %0, -%1, %2, %3" : "=v"(r) : "v"(a), "v"(b), "v"(c)); return r; }
__device__ __forceinline__ f32x2 cmul(f32x2 a, f32x2 b) { return (f32x2){fnma_(a.y, b.y, fmul_(a.x, b.x)), ffma_(a.y, b.x, fmul_(a.x, b.y))}; }
__device__ __forceinline__ f32x2 cmlc(f32x2 a, f32x2 b) { return (f32x2){ffma_(a.y, b.y, fmul_(a.x, b.x)), fnma_(a.x, b.y, fmul_(a.y, b.x))}; }
__device__ __forceinline__ float ssm_par(const u32x4 a, unsigned ldtv, int h, f32x4& p) {
  const float dt = rbf(__expf(__uint_as_float(ldtv << 16)));
  float mx = fmaxf(fmaxf(fabsf(bflo(a[2])), fabsf(bfhi(a[2]))), fmaxf(fabsf(bflo(a[3])), fabsf(bfhi(a[3]))));
  mx = dppmax<0x128>(mx); mx = dppmax<0x124>(mx); mx = dppmax<0x122>(mx); mx = dppmax<0x121>(mx);
  const unsigned mb = __float_as_uint(mx);
  const float mw = fmaxf(fmaxf(__uint_as_float(__builtin_amdgcn_readlane(mb, 0)), __uint_as_float(__builtin_amdgcn_readlane(mb, 16))),
                         fmaxf(__uint_as_float(__builtin_amdgcn_readlane(mb, 32)), __uint_as_float(__builtin_amdgcn_readlane(mb, 48))));
  const int ex = (mw > 0.f && mw < 3.0e38f) ? __builtin_amdgcn_frexp_expf(mw) : 0;
  const int sh = h ? 0 : 16;
  const float la = bfhi(a[0] << sh), ai = bfhi(a[1] << sh), cr = bfhi(a[2] << sh), ci = bfhi(a[3] << sh);
  p = (f32x4){-rbf(__expf(la)) * dt * 1.44269504f, ai * dt * 0.15915494f, __builtin_amdgcn_ldexpf(cr, -ex), __builtin_amdgcn_ldexpf(ci, -ex)};
  return __builtin_amdgcn_ldexpf(2.f * dt, ex);
}
__device__ __forceinline__ f16x2 tohf(f32x2 v) { return __builtin_convertvector(v, f16x2); }
__device__ __forceinline__ f16x2 hmul(f16x2 a, f16x2 b) { return __builtin_elementwise_fma((f16x2){-b.y, b.x}, (f16x2){a.y, a.y}, b * (f16x2){a.x, a.x}); }
__device__ __forceinline__ f16x2 hmlc(f16x2 a, f16x2 b) { return __builtin_elementwise_fma((f16x2){a.y, -a.x}, (f16x2){b.y, b.y}, a * (f16x2){b.x, b.x}); }
__device__ __forceinline__ void ssm_kgen(const f32x4 p, LAS char* kb, int lane, f32x4 (&kd)[3]) {
  int tb = 0;
  auto put = [&](int row, f16x2 v) { *(LAS f16x2*)(kb + tb + row * 272 + lane * 4) = v; };
  LAS char* rp = kb + (lane & 15) * 272 + (lane >> 4) * 16;
  f32x2 w[12];
#pragma unroll
  for (int k = 0; k < 12; ++k) {
    if (k % 3 == 0) { const float sc = (float)(1 << k), e = __builtin_amdgcn_exp2f(p[0] * sc), ph = __builtin_amdgcn_fractf(p[1] * sc);
      w[k] = (f32x2){e * __builtin_amdgcn_cosf(ph), e * __builtin_amdgcn_sinf(ph)}; }
    else { const f32x2 v = w[k - 1]; w[k] = (f32x2){fnma_(v.y, v.y, fmul_(v.x, v.x)), fmul_(v.x + v.x, v.y)}; } }
  f32x2 y[8];
  y[0] = (f32x2){p[2], -p[3]}; y[1] = cmlc(y[0], w[3]); y[2] = cmlc(y[0], w[4]); y[3] = cmlc(y[1], w[4]);
#pragma unroll
  for (int b = 0; b < 4; ++b) y[4 + b] = cmlc(y[b], w[5]);
  const f16x2 h6 = tohf(w[6]), h7 = tohf(w[7]), h10 = tohf(w[10]);
#pragma unroll
  for (int b = 0; b < 8; ++b) { const f16x2 hy = tohf(y[b]); put(b, hy); put(8 + b, hmlc(hy, h6)); }
  f16x8 fb[4];
  auto tile = [&](int t, int tab) { kd[t] = (f32x4){0, 0, 0, 0};
#pragma unroll
    for (int k = 0; k < 4; ++k) kd[t] = __builtin_amdgcn_mfma_f32_16x16x32_f16(*(LAS f16x8*)(rp + tab + k * 64), fb[k], kd[t], 0, 0, 0); };
  tb = 4352;
  f16x2 g[8];
  { const f32x2 g3 = cmul(w[0], w[1]);
    g[0] = (f16x2){(_Float16)1.f, (_Float16)0.f}; g[1] = tohf(w[0]); g[2] = tohf(w[1]); g[3] = tohf(g3); g[4] = tohf(w[2]);
    g[5] = tohf(cmul(w[2], w[0])); g[6] = tohf(cmul(w[2], w[1])); g[7] = tohf(cmul(w[2], g3)); }
#pragma unroll
  for (int c = 0; c < 8; ++c) { put(c, g[c]); put(8 + c, c ? hmul(g[c], h7) : h7); }
#pragma unroll
  for (int k = 0; k < 4; ++k) fb[k] = *(LAS f16x8*)(rp + k * 64);
  tile(0, 4352);
  tb = 0;
  f16x2 f[8];
  { const f32x2 f3 = cmul(w[8], w[7]);
    f[1] = h7; f[2] = tohf(w[8]); f[3] = tohf(f3); f[4] = tohf(w[9]); f[5] = tohf(cmul(w[9], w[7])); f[6] = tohf(cmul(w[9], w[8])); f[7] = tohf(cmul(w[9], f3)); }
#pragma unroll
  for (int a = 0; a < 2; ++a)
#pragma unroll
    for (int c = 0; c < 4; ++c) put(4 * a + c, c ? hmul(f[2 + a], g[2 * c]) : f[2 + a]);
#pragma unroll
  for (int a = 0; a < 4; ++a) { put(8 + 2 * a, f[4 + a]); put(9 + 2 * a, hmul(f[4 + a], g[4])); }
  tb = 4352;
#pragma unroll
  for (int a = 0; a < 8; ++a) put(a, a ? hmul(h10, f[a]) : h10);
  put(8, tohf(w[11]));
  tile(1, 0); tile(2, 4352);
}

struct ZF { bf16x8 x[4]; s16x4 w[5][2]; };

__global__ __launch_bounds__(512, 2) void gmix_fused(const u16* __restrict__ x, const u16* __restrict__ lar, const u16* __restrict__ aim, const u16* __restrict__ cre,
    const u16* __restrict__ cim, const u16* __restrict__ ldt, const u16* __restrict__ dsk, const u16* __restrict__ win, const u16* __restrict__ wsp,
    const u16* __restrict__ bsp, const u16* __restrict__ wout, const u16* __restrict__ gw, u16* __restrict__ out, char* ws) {
  extern __shared__ __attribute__((aligned(16))) char smem_[];
  LAS char* smem = (LAS char*)smem_;
  const int wid = __builtin_amdgcn_readfirstlane(threadIdx.x >> 6);
  int lane = threadIdx.x & 63;
  const int bx = blockIdx.x & 7, bl = blockIdx.x >> 3;
  const int tm = (bx >> 2) * 8 + (bl & 7), tn = (bx & 3) * 4 + (bl >> 3);
  const int pp = tn >> 1, ee = tn & 1;
  const int t3 = bx * 4 + (bl & 3), dn = bl >> 2;
  unsigned* ctl = (unsigned*)(ws + OFF_CTL);
  int fr = lane & 15, fq = lane >> 4, v16 = lane * 16;
  const bool ssm = !(tm & 1) || tm == 1;
  const int ts = tm == 1 ? 8 : tm >> 1;
  const int wcol = (288 * tn) & ~127;
  const int uP = ((tm - 3) >> 1) * 16 + tn;
  if (CTOUCH && wid == 6) {
    const char* pc = (const char*)(__builtin_amdgcn_s_getpc() & ~127ull);
    if (lane < CLN) touch_asm(pc + (bl * CLN + lane) * 128);
  }
  u32x4 sj = {0, 0, 0, 0}; unsigned pdt2 = 0;
  const bool isL = wid == 4 || wid == 5;
  unsigned nonce = 0;
  if (wid < 4 && ssm) { const int d = 36 * tn + 4 * ts + wid, o = d * 64 + lane;
    sj = (u32x4){((const unsigned*)lar)[o], ((const unsigned*)aim)[o], ((const unsigned*)cre)[o], ((const unsigned*)cim)[o]};
    asm volatile("" : "+v"(sj));
  }
  { const int d = ssm ? 36 * tn + 4 * ts + (wid & 3) : 0; pdt2 = (((const unsigned*)ldt)[d >> 1] >> (16 * (d & 1))) & 0xffffu; }
  const int lw = wid & 1;
  const rsrc_t rX = mkrs(x + (size_t)tm * 128 * 576, 128 * 1152);
  const rsrc_t rWi = mkrs(win, 576 * 9216);
  int vxP[2], vxN[2], vw[6];
  auto zaddr = [&](int ln) {
#pragma unroll
    for (int h = 0; h < 2; ++h) {
      const int swz = ((ln & 7) ^ ((4 * h + (ln >> 4)) & 7)) << 4;
      const int i = 8 * h + (ln >> 3);
      vxP[h] = (8 * (i >> 2) + (i & 3)) * 1152 + swz;
      vxN[h] = i * 1152 + swz;
    }
#pragma unroll
    for (int q = 0; q < 6; ++q) { const int o = q * 1024 + ln * 16, row = o / 384, pos = (o - row * 384) >> 4; vw[q] = row * 9216 + ((pos ^ ((row & 2) | ((row & 8) >> 1))) << 4); }
  };
  auto relane = [&]() { int z = 0; asm volatile("" : "+v"(z));
    lane = __builtin_amdgcn_mbcnt_hi(~0u, __builtin_amdgcn_mbcnt_lo(~0u, z)); fr = lane & 15; fq = lane >> 4; v16 = lane * 16; };
  auto issueZ = [&](int g, int slot) {
    const bool z2 = g < 9; const int kt = z2 ? g : g - 9;
    LAS char* dst = smem + slot;
    const int sw = kt * 64 * 9216 + wcol + (z2 ? 4608 : 0);
    if (lw == 0) {
      const int v0 = z2 ? vxP[0] : vxN[0], v1 = z2 ? vxP[1] : vxN[1], odd = (z2 ? 4 : 16) * 1152;
#pragma unroll 1
      for (int t2 = 0; t2 < 4; ++t2) { const int so = t2 * 32 * 1152 + kt * 128; LAS char* d = dst + t2 * 4096;
        dma16(rX, d, v0, so); dma16(rX, d + 1024, v1, so); dma16(rX, d + 2048, v0, so + odd); dma16(rX, d + 3072, v1, so + odd); }
#pragma unroll
      for (int q = 0; q < 4; ++q) dma16(rWi, dst + 16384 + q * 1024, vw[q], sw);
    } else {
      dma16(rWi, dst + 16384 + 4096, vw[4], sw); dma16(rWi, dst + 16384 + 5120, vw[5], sw);
#pragma unroll 1
      for (int q6 = 1; q6 < 4; ++q6)
#pragma unroll
        for (int q = 0; q < 6; ++q) dma16(rWi, dst + 16384 + (q6 * 6 + q) * 1024, vw[q], sw + q6 * 16 * 9216);
    }
  };
  if (isL && LPRIO) __builtin_amdgcn_s_setprio(LPRIO);
  if (wid >= 4) {
    const int o = (ssm ? 36 * tn + 4 * ts + (wid & 3) : 0) * 64 + lane;
    unsigned a0, a1, a2, a3, nv;
    asm volatile("global_load_dword %0, %5, off\n\tglobal_load_dword %1, %6, off\n\tglobal_load_dword %2, %7, off\n\tglobal_load_dword %3, %8, off\n\tglobal_load_dword %4, %9, off sc1"
                 : "=&v"(a0), "=&v"(a1), "=&v"(a2), "=&v"(a3), "=&v"(nv)
                 : "v"((const unsigned*)lar + o), "v"((const unsigned*)aim + o), "v"((const unsigned*)cre + o), "v"((const unsigned*)cim + o), "v"(ctl + C_R + blockIdx.x) : "memory");
    zaddr(lane);
    SB0();
    if (!isL) __builtin_amdgcn_s_sleep(HSLP);
    const u32x4 aX = mkrs4(x + (size_t)tm * 128 * 576, 128 * 1152), aW = mkrs4(win, 576 * 9216);
    const int kt = wid >> 1 & 1; const unsigned dst = kt * ZST; const int sw = kt * 64 * 9216 + wcol + 4608;
    const u32x4 aA = lw ? aW : aX;
#pragma unroll
    for (int p = 0; p < 16; ++p) dma16_asm(aA, dst + (lw ? 16384 + (p + 8) * 1024 : p * 1024), lw ? vw[(p + 8) % 6] : vxP[p & 1],
                                           lw ? sw + ((p + 8) / 6) * 16 * 9216 : ((p >> 2) * 32 + (p >> 1 & 1) * 4) * 1152 + kt * 128);
#pragma unroll
    for (int q = 0; q < 4; ++q) dma16_asm(aW, dst + 16384 + (q + 4 * lw) * 1024, lw ? vw[(q + 4) % 6] : vw[q], sw + (lw && q >= 2 ? 16 * 9216 : 0));
    const bool isx = lane < 32 || lane >= 56; const int l3 = (lane - 32) / 3, k0 = 2 + (wid - 4);
    const char* tsh = isx ? (const char*)x + (size_t)(tm * 128 + (bl >> 3) * 32 + (lane & 31)) * 1152 + k0 * 128
                          : (const char*)win + (size_t)(64 * k0 + 8 * (bl & 7) + l3) * 9216 + wcol + 4608 + (lane - 32 - 3 * l3) * 128;
    if (NT0) { touch_asm(tsh); touch_asm(tsh + (size_t)(wid == 7 ? 3 : 4) * (isx ? 128 : 64 * 9216)); }
    asm volatile("s_waitcnt vmcnt(%5)" : "+v"(a0), "+v"(a1), "+v"(a2), "+v"(a3), "+v"(nv) : "i"(20 + NT0) : "memory");
    sj = (u32x4){a0, a1, a2, a3};
    nonce = __builtin_amdgcn_readfirstlane(nv) + 1u;
    if (wid == 7 && lane == 0) __hip_atomic_store((GAS unsigned*)ctl + C_R + blockIdx.x, nonce, RLX);
  }
  if (ssm) {
    f32x4 p, kd[3]; const float ksc = ssm_par(sj, pdt2, wid >> 2, p);
    LAS char* kb = smem + 2 * ZST + wid * 8704;
    ssm_kgen(p, kb, lane, kd);
#pragma unroll
    for (int t = 0; t < 3; ++t) *(LAS f32x4*)(kb + t * 1024 + v16) = kd[t] * ksc;
  }
  WAITL0(); BAR();
  u32x4 wrg[JR * 6];
  auto wreg_load = [&](int wi) {
    const rsrc_t rF = mkrs(ws + OFF_WF + (size_t)dn * 72 * 4608 + (18 - JR) * 18432 + wi * (JR * 6144), JR * 6144);
#pragma unroll
    for (int k = 0; k < JR * 6; ++k) wrg[k] = __builtin_amdgcn_raw_buffer_load_b128(rF, v16, k * 1024, 16); };
  auto wreg_dump = [&](int wi) {
#pragma unroll
    for (int k = 0; k < JR * 6; ++k) *(LAS u32x4*)(smem + GST + wi * (JR * 6144) + k * 1024 + v16) = wrg[k];
    WAITL0(); };
  auto kcopy = [&](int c, int kb0, int n, bool head) {
    LAS char* kx = smem + L_KX + c * 4096 + 8 * lane + 512 * kb0; LAS char* cv = smem + l_kcp(c) + 64 + 8 * lane + 512 * kb0;
#pragma unroll
    for (int k = 0; k < n; ++k) { const u32x2 v0 = *(LAS u32x2*)(kx + 512 * k), v1 = *(volatile LAS u32x2*)(kx + 512 * k + 8); const unsigned s1 = __builtin_amdgcn_alignbit(v1[0], v0[1], 16);
      *(LAS u32x2*)(cv + cb0 + 512 * k) = v0; *(LAS u32x2*)(cv + cb1 + 512 * k) = (u32x2){__builtin_amdgcn_alignbit(v0[1], v0[0], 16), s1};
      *(LAS u32x2*)(cv + cb2 + 512 * k) = (u32x2){v0[1], v1[0]}; *(LAS u32x2*)(cv + cb3 + 512 * k) = (u32x2){s1, __builtin_amdgcn_alignbit(v1[1], v1[0], 16)}; }
    if (head) { const u32x2 kq = *(LAS u32x2*)(smem + L_KX + c * 4096); const unsigned q0 = kq[0] << 16; const bool l7 = lane == 7;
      if (lane < 8) {
        *(LAS u32x2*)(cv - 64 + cb0) = (u32x2){0u, 0u}; *(LAS u32x2*)(cv - 64 + cb1) = (u32x2){0u, l7 ? q0 : 0u};
        *(LAS u32x2*)(cv - 64 + cb2) = (u32x2){0u, l7 ? kq[0] : 0u}; *(LAS u32x2*)(cv - 64 + cb3) = (u32x2){l7 ? q0 : 0u, l7 ? __builtin_amdgcn_alignbit(kq[1], kq[0], 16) : 0u}; } }
  };

  if (wid >= 6) {
    relane();
    if (HPRIO) __builtin_amdgcn_s_setprio(HPRIO);
    const int hw = wid - 6;
    const int uR = hw ? 112 + uP : uP;
    const bool retile = !ssm && uR < 144;
    u32x2 kd[2][3];
#pragma unroll
    for (int c = 0; c < 2; ++c)
#pragma unroll
      for (int t = 0; t < 3; ++t) { LAS char* kp = smem + 2 * ZST + (2 * hw + c) * 8704 + t * 1024 + v16; const f32x4 v = *(LAS f32x4*)kp + *(LAS f32x4*)(kp + 4 * 8704);
        kd[c][t] = (u32x2){pk2(v[0], v[1]), pk2(v[2], v[3])}; asm volatile("" : "+v"(kd[c][t])); }
    const bool leader = blockIdx.x == 0 && hw == 1;
    const rsrc_t rC = mkrs(ctl, 4096);
    auto echo = [&](const u32x4 r) {
      st128((u32x4){r[0], nonce, r[1], nonce}, rC, lane * 32 + (C_S * 4));
      st128((u32x4){r[2], nonce, r[3], nonce}, rC, lane * 32 + 16 + (C_S * 4)); };
    BAR();
    if (ssm) {
#pragma unroll
      for (int c = 0; c < 2; ++c)
#pragma unroll
        for (int t = 0; t < 3; ++t) if (t < 2 || lane < 40) asm volatile("ds_write_b64 %0, %1" :: "v"(L_KC + (2 * hw + c) * 1344 + t * 512 + lane * 8), "v"(kd[c][t]) : "memory");
    }
    if (hw == 0) {
      int zs = 0;
#pragma unroll 1
      for (int g = 0; g < 9; ++g) {
        if (g == 0) WAITV(0);
        if (g == (9 * tn) >> 4 || g == (9 * tn + 8) >> 4) {
          const rsrc_t rXT = mkrs(ws + OFF_XT + (size_t)(tn * 64 + tm * 4) * 2304, 4 * 2304);
          const int q4 = fr >> 2, p4 = lane & 3;
#pragma unroll
          for (int ct = 0; ct < 3; ++ct) { const int lq = (9 * tn + 4 * ct + p4) & 15, myq = 4 * ct + (fr >> 2); u32x2 h[4][2];
#pragma unroll
            for (int rg = 0; rg < 4; ++rg)
#pragma unroll
              for (int e = 0; e < 2; ++e) { const int rho = 16 * (1 - e) + 4 * (3 - fq) + 3 - q4;
                asm volatile("ds_read_b64_tr_b16 %0, %1 offset:%2" : "=v"(h[rg][e]) : "v"(zs + rho * 128 + (((lq >> 1) ^ ((rho >> 1) & 7)) << 4) + 8 * (lq & 1)), "i"(rg * 4096) : "memory"); }
            asm volatile("s_waitcnt lgkmcnt(0)" : "+v"(h[0][0]), "+v"(h[0][1]), "+v"(h[1][0]), "+v"(h[1][1]), "+v"(h[2][0]), "+v"(h[2][1]), "+v"(h[3][0]), "+v"(h[3][1]) :: "memory");
            if (myq < 9 && ((9 * tn + myq) >> 4) == g) {
#pragma unroll
              for (int rg = 0; rg < 4; ++rg) st128((u32x4){h[rg][0][0], h[rg][0][1], h[rg][1][0], h[rg][1][1]}, rXT, rg * 2304 + (16 * ct + fr) * 64 + fq * 16); } }
        }
        if (g == 2) {
          __builtin_amdgcn_global_load_lds((const GAS unsigned*)(bsp + tm * 128 + 2 * lane), (LAS unsigned*)(smem + L_BIAS), 4, 0, 0); }
        BAR();
        zs = (zs == 2 * ZST) ? 0 : zs + ZST;
      }
    } else {
      WAITV(0); BAR(); BAR();
      const int grow = tm * 128 + 8 * tn;
      u32x4 gx[8][2], gg[2];
#pragma unroll
      for (int k = 0; k < 2; ++k) { const int c = k ? 64 + (lane & 7) : lane; gg[k] = *(const u32x4*)(gw + c * 8);
#pragma unroll
        for (int r = 0; r < 8; ++r) gx[r][k] = *(const u32x4*)(x + (size_t)(grow + r) * 576 + c * 8); }
      SB0(); BAR(); SB0();
      const u32x4 rq = __builtin_amdgcn_raw_buffer_load_b128(mkrs(ctl, leader ? 1024 : 0), v16, C_R * 4, 16);
      SB0(); BAR(); SB0();
      float sr[8];
#pragma unroll
      for (int r = 0; r < 8; ++r) { float s0 = 0.f, s1 = 0.f;
#pragma unroll
        for (int j = 0; j < 4; ++j) { s0 = dot2bf(gx[r][0][j], gg[0][j], s0); s1 = dot2bf(gx[r][1][j], gg[1][j], s1); }
        sr[r] = s0 + (lane < 8 ? s1 : 0.f); asm volatile("" : "+v"(sr[r]));
        if (r == 3) { SB0(); BAR(); SB0(); if (leader) echo(rq); } }
      SB0(); BAR(); SB0();
      float sv8 = 0.f;
#pragma unroll
      for (int r = 0; r < 8; ++r) { sr[r] = rowsum16(sr[r]); sv8 = fr == r ? sr[r] : sv8; }
      sv8 += __shfl_xor(sv8, 16); sv8 += __shfl_xor(sv8, 32);
      if (lane < 8) __hip_atomic_store((GAS unsigned*)(ws + OFF_GATE) + grow + lane, __float_as_uint(__builtin_amdgcn_rcpf(1.f + __builtin_amdgcn_exp2f(-1.44269504f * sv8))), RLX);
      SB0(); BAR(); SB0();
      BAR(); BAR();
    }
    if (hw == 1) {
      unsigned ep = nonce;
      if (!leader) {
        for (unsigned sp = 0; sp < SPIN_CAP; ++sp) {
          const unsigned long long v = __hip_atomic_load((GAS unsigned long long*)(ctl + C_S) + blockIdx.x, RLX);
          ep = (unsigned)(v >> 32);
          if ((unsigned)v == nonce) break;
          __builtin_amdgcn_s_sleep(2);
        }
      }
      if (lane == 0) { *(LAS unsigned*)(smem + L_EPO) = ep; __hip_atomic_store((GAS unsigned*)ctl + C_A + blockIdx.x, ep, RLX); }
      WAITL0();
    }
    const int uK = uR >> 1, uH = uR & 1, p9 = uK / 9, r9 = uK - p9 * 9; const bool nat = r9 == 8;
    LAS char* rs = smem + L_RT + hw * 18432;
    if (retile) {
      const rsrc_t rO = mkrs(wout, 2304 * 1152);
#pragma unroll 2
      for (int k = 0; k < 18; ++k) { const int c = k * 64 + lane, row = c / 36, ch = c - row * 36;
        const int f = nat ? 144 * (2 * p9 + (row >> 4)) + 128 + (row & 15) : 144 * (2 * p9 + (r9 >> 2)) + 32 * (r9 & 3) + row;
        dma16(rO, rs + k * 1024, f * 1152 + uH * 576 + ch * 16, 0); }
    }
    const char* sv = (const char*)wsp + (size_t)(blockIdx.x * 2 + hw) * 16384 + lane * 128;
    { const char* wv = (const char*)win + (size_t)(192 + (bl & 7) * 48 + hw * 24 + (lane >> 2)) * 9216 + wcol + ((lane & 3) < 3 ? (lane & 3) : 2) * 128;
      touch_asm(wv); if (lane < 32) touch_asm(wv + 16 * 9216);
      if (!retile) touch_asm(sv); }
    BAR(); BAR();
    const unsigned E = __builtin_amdgcn_readfirstlane(*(volatile LAS unsigned*)(smem + L_EPO));
    {
      const rsrc_t rS = mkrs(ws + OFF_ST + (size_t)tm * 16 * 2048, 16 * 2048);
      u32x4 gr[16];
      auto ldgr = [&]() {
#pragma unroll
        for (int k = 0; k < 16; ++k) gr[k] = __builtin_amdgcn_raw_buffer_load_b128(rS, (64 * hw + lane) * 16, k * 2048, 16); };
      u32x4 r = __builtin_amdgcn_raw_buffer_load_b128(mkrs(ctl, leader ? 4096 : 0), v16, C_R * 4, 16), a = __builtin_amdgcn_raw_buffer_load_b128(mkrs(ctl, leader ? 4096 : 0), v16, C_A * 4, 16);
      SB0(); BAR(); SB0();
      if (RNG == 12) ldgr();
      if (leader) {
        for (unsigned sp = 0; sp < SPIN_CAP; ++sp) {
          if (__all(a[0] == nonce && a[1] == nonce && a[2] == nonce && a[3] == nonce)) break;
          echo(r);
          __builtin_amdgcn_s_sleep(2);
          r = __builtin_amdgcn_raw_buffer_load_b128(rC, v16, C_R * 4, 16); a = __builtin_amdgcn_raw_buffer_load_b128(rC, v16, C_A * 4, 16);
        }
      }
      if (RNG == 14) { SB0(); BAR(); SB0(); }
      if (RNG >= 13) { if (GSLP >= 0) { __builtin_amdgcn_s_sleep(GSLP < 0 ? 0 : GSLP); ldgr(); } SB0(); BAR(); SB0(); if (GSLP < 0) ldgr(); }
      float s, q;
      for (unsigned sp = 0;; ++sp) {
        bool ok = true; s = 0.f; q = 0.f;
#pragma unroll
        for (int k = 0; k < 16; ++k) { ok = ok && gr[k][1] == E && gr[k][3] == E; s += __uint_as_float(gr[k][0]); q += __uint_as_float(gr[k][2]); }
        if (__all(ok) || sp >= SPIN_CAP) break;
        __builtin_amdgcn_s_sleep(1);
        ldgr();
      }
      const float mean = s * (1.f / 2304.f), var = fmaxf(q * (1.f / 2304.f) - mean * mean, 0.f), rs = __builtin_amdgcn_rsqf(var + 1e-5f);
      *(LAS float*)(smem + L_RN + (64 * hw + lane) * 4) = rs; *(LAS float*)(smem + L_RN + 512 + (64 * hw + lane) * 4) = -mean * rs;
      WAITL0(); BAR();
    }
    WAITV(0);
#pragma unroll 1
    for (int dl = 0; dl < 4; ++dl) {
      if (retile) {
        const int q4 = fr >> 2, p4 = lane & 3;
        const rsrc_t rF = mkrs(ws + OFF_WF, 2654208);
#pragma unroll
        for (int c = 0; c < 5; ++c) { s16x4 h[2];
#pragma unroll
          for (int t = 0; t < 2; ++t) { const int row = nat ? 8 * fq + 4 * t + q4 : 16 * t + 4 * fq + q4; int col = 72 * dl + 16 * c + 4 * p4; col = col > 284 ? 284 : col;
            h[t] = __builtin_amdgcn_ds_read_tr16_b64_v4i16((LAS s16x4*)(rs + row * 576 + col * 2)); }
          const u32x4 v = __builtin_bit_cast(u32x4, __builtin_shufflevector(h[0], h[1], 0, 1, 2, 3, 4, 5, 6, 7));
          const int so = ((4 * uH + dl) * 72 + uK) * 4608 + c * 1024;
          if (c < 4) st128(v, rF, v16 + (so));
          else if (fr < 8) st128(v, rF, (fq * 8 + fr) * 16 + (so));
        }
        WAITL0();
      }
      if (RNG < 14 || dl > 0) BAR();
      if (dl == (RNG == 14 ? 2 + hw : 1 + 2 * hw)) touch_asm(sv + 8192);
      if (dl == 2 * hw && retile && RNG == 12) touch_asm(sv);
      if (dl == (RNG == 14 ? 3 - hw : 2) && retile && RNG >= 13) touch_asm(sv);
    }
    if (RNG == 12) BAR();
    if (ssm) {
      auto ka0 = [](int i) { return (((i >> 3) & 15) + 16 * (2 * (i >> 7) + ((i >> 2) & 1))) * 8; };
      auto ka1 = [](int u) { return 512 + (((u >> 2) & 15) + 16 * (u >> 6)) * 8 + (u & 3) * 2; };
      auto ka2 = [](int v) { return v < 128 ? 768 + (((v >> 1) & 15) + 16 * (v >> 6)) * 8 + (((v >> 5) & 1) * 2 + (v & 1)) * 2 : 1024; };
      auto ka3 = [](int w) { return 1024 + ((w & 15) + 16 * (w >> 6)) * 8 + ((w >> 4) & 3) * 2; };
      const int na = (0x0415 >> (4 * (lane & 3))) & 15;
      const unsigned s2 = (lane & 1) ? 0x03020302u : 0x03020100u, s30 = (0 < na ? 0x0100u : 0x0302u) | (1 < na ? 0x01000000u : 0x03020000u), s32 = 2 < na ? 0x01000100u : 0x03020302u;
      LAS char* kb = smem + L_KC + 2 * hw * 1344;
      LAS char* kx = smem + L_KX + 2 * hw * 4096 + 8 * lane;
      u32x2 q[2]; unsigned lo[8][2], hi[8][2];
#pragma unroll
      for (int k = 0; k < 8; ++k) {
        const int v = 64 * (k - 2) + lane, w = 32 * (k - 4) + (lane >> 1);
        const int a0 = k == 0 ? ka0(4 * lane) : k == 1 ? ka1(2 * lane) : k < 4 ? ka2(v) : ka3(w), a1 = k == 1 ? (lane == 63 ? 768 : ka1(2 * lane + 2)) : k < 4 ? ka2(v + 1) : ka3(w + 1);
#pragma unroll
        for (int c = 0; c < 2; ++c) { if (k == 0) q[c] = *(LAS u32x2*)(kb + c * 1344 + a0);
          else { lo[k][c] = k == 1 ? *(LAS unsigned*)(kb + c * 1344 + a0) : *(LAS u16*)(kb + c * 1344 + a0); hi[k][c] = *(LAS u16*)(kb + c * 1344 + a1); } }
      }
#pragma unroll
      for (int k = 0; k < 8; ++k)
#pragma unroll
        for (int c = 0; c < 2; ++c) { const unsigned ab = lo[k][c] | (hi[k][c] << 16);
          *(LAS u32x2*)(kx + c * 4096 + 512 * k) = k == 0 ? q[c] : k == 1 ? (u32x2){__builtin_amdgcn_perm(lo[k][c], lo[k][c], 0x01000100u), __builtin_amdgcn_alignbit(hi[k][c], lo[k][c], 16)}
              : (u32x2){__builtin_amdgcn_perm(ab, ab, k < 4 ? 0x01000100u : s30), __builtin_amdgcn_perm(ab, ab, k < 4 ? s2 : s32)}; }
      WAITL0();
    }
    BAR(); BAR();
    WAITV(0);
    if (retile && lane == 0) __hip_atomic_store((GAS unsigned*)ctl + C_FW + uR, E, RLX);
#pragma unroll 1
    for (int i = 1; i < 18; ++i) BAR();
    if (hw == 0) {
      for (unsigned sp = 0; sp < SPIN_CAP; ++sp) {
        const u32x4 f = __builtin_amdgcn_raw_buffer_load_b128(mkrs(ctl + C_FW, 576), v16, 0, 16);
        if (__all(lane >= 36 || (f[0] == E && f[1] == E && f[2] == E && f[3] == E))) break;
        __builtin_amdgcn_s_sleep(8);
      }
    }
    const char* fv = ws + OFF_WF + (size_t)dn * 72 * 4608 + (size_t)(bl & 3) * 82944 + lane * 128;
#pragma unroll 1
    for (int i = 18; i < 31; ++i) { BAR(); if (hw == 0 && i >= 31 - WTN) { touch_asm_c(fv); fv += 8192; } }
    BAR();
    if (ssm) kcopy(2 + hw, 0, 8, true);
    WAITL0(); BAR();
    __builtin_amdgcn_s_sleep(WSLP);
    if (hw == 0) {
#pragma unroll
      for (int k = 0; k < 10 - WTN; ++k) touchc(fv + k * 8192);
      if (lane < 8) touchc(fv + (10 - WTN) * 8192);
      WAITV(0);
      const rsrc_t rF = mkrs(ws + OFF_WF + (size_t)dn * 72 * 4608, 72 * 4608);
#pragma unroll 2
      for (int p = 0; p < JL * 18; ++p) dma16c(rF, smem + l_wpf(0) + p * 1024, v16, p * 1024);
    }
    wreg_load(hw);
    WAITV(0);
#pragma unroll
    for (int k = 0; k < JR * 6; ++k) asm volatile("" : "+v"(wrg[k]));
    BAR();
    wreg_dump(hw);
    return;
  }

  if (wid >= 4) {
    relane(); zaddr(lane);
    {
      WAITV(NT0);
      BAR();
      issueZ(2, 2 * ZST);
      int zs = 0;
#pragma unroll 1
      for (int g = 0; g < 15; ++g) { WAITV(20); BAR(); issueZ(g + 3, zs); zs = (zs == 2 * ZST) ? 0 : zs + ZST; }
    }
    const unsigned E = __builtin_amdgcn_readfirstlane(*(volatile LAS unsigned*)(smem + L_EPO));
    const rsrc_t rW = mkrs(wsp + (size_t)tm * 128 * 2048, 128 * 4096);
    const rsrc_t rZ = mkrs(ws + OFF_Z2, SZ_F);
    const int vW0 = (lane >> 3) * 4096 + (((lane & 7) ^ (lane >> 4)) << 4), vW1 = (lane >> 3) * 4096 + (((lane & 7) ^ (4 + (lane >> 4))) << 4);
    auto issueW = [&](int ki, int slot) {
      const int kt = (ki + ROT * 2 * tm) & 31;
      LAS char* dst = smem + slot * GST;
#pragma unroll
      for (int p = 0; p < 16; ++p) __builtin_amdgcn_raw_ptr_buffer_load_lds(rW, (LAS void*)(dst + p * 1024), 16, (p & 1) ? vW1 : vW0, p * 8 * 4096 + kt * 128, 0, AUXS);
    };
    unsigned long long rdy = 0;
    auto issueZ2 = [&](int ki, int slot) {
      const int kt = (ki + ROT * 2 * tm) & 31;
      const unsigned long long need = ROT ? 15ull << (4 * (kt >> 1)) : ~0ull;
      for (unsigned sp = 0; (rdy & need) != need && sp < SPIN_CAP; ++sp) {
        if (sp) __builtin_amdgcn_s_sleep(1);
        rdy = __ballot(__hip_atomic_load((GAS unsigned*)ctl + C_F2 + tn * 64 + lane, RLX) == E);
      }
      asm volatile("" ::: "memory");
      LAS char* dst = smem + slot * GST + 16384; const int so = (kt * 144 + 9 * tn) * 2048;
#pragma unroll
      for (int p = 0; p < 18; ++p) __builtin_amdgcn_raw_ptr_buffer_load_lds(rZ, (LAS void*)(dst + p * 1024), 16, v16, so + p * 1024, 0, AUXZ);
    };
    if (lw == 0) {
      WAITV(20); BAR(); issueW(0, 0);
      WAITV(16); BAR(); issueW(1, 1);
      BAR(); issueW(2, 2); issueW(3, 3);
      WAITV(48); BAR();
#pragma unroll 1
      for (int i = 0; i < 28; ++i) { WAITV(32); BAR(); issueW(i + 4, i & 3); }
      int vu[8];
#pragma unroll
      for (int i = 0; i < 8; ++i) { const int n = 64 * i + lane, blk = n / 6, ck = n - 6 * blk; vu[i] = (blk >= 15 && blk < 79 && ck < 4) ? (blk - 15) * 2304 + ck * 16 : 0x40000000; }
      WAITV(32); BAR();
      if (ssm) {
        const rsrc_t rU = mkrs(ws + OFF_XT + (size_t)tn * 64 * 2304, 64 * 2304);
#pragma unroll 1
        for (int c = 0; c < 4; ++c)
#pragma unroll
          for (int i = 0; i < 8; ++i) dma16c(rU, smem + L_US + c * UCP + i * 1024, vu[i], (4 * ts + c) * 64);
        WAITV(48); BAR(); WAITV(32); BAR();
      } else { WAITV(16); BAR(); WAITV(0); BAR(); }
      WAITV(0); BAR();
    } else {
      WAITV(20); BAR();
      WAITV(0); BAR();
      BAR();
#pragma unroll 1
      for (int i = 0; i < 2; ++i) issueZ2(i, i);
      WAITV(18); BAR();
      issueZ2(2, 2);
      WAITV(18); BAR();
      issueZ2(3, 3); issueZ2(4, 0);
#pragma unroll 1
      for (int i = 1; i < 28; ++i) { WAITV(36); BAR(); issueZ2(i + 4, i & 3); }
      WAITV(36); BAR();
      WAITV(18); BAR();
      WAITV(0); BAR();
      BAR();
    }
    if (ssm) kcopy(lw, 0, 8, true);
    WAITL0(); BAR();
    if (lw == 0) { __builtin_amdgcn_s_sleep(WSLP); wreg_load(2);
#pragma unroll
      for (int k = 0; k < JR * 6; ++k) asm volatile("" : "+v"(wrg[k])); }
    if (lw == 1) {
      __builtin_amdgcn_s_sleep(F3SLP);
      wait_flags(ctl + C_F3 + (t3 >> 1) * 64 + lane, true, E);
      { const char* hv = ws + OFF_H + (size_t)t3 * 4 * 73728 + (size_t)dn * 36864 + lane * 128;
#pragma unroll
        for (int k = 0; k < 4; ++k) touchc(hv + k * 8192);
        if (lane < 32) touchc(hv + 4 * 8192); }
    }
    BAR();
    if (lw == 0) {
      wreg_dump(2);
      BAR();
      wait_flags<8>(ctl + C_FA + dn * 64 + lane, lane < 18, E);
      const rsrc_t rA = mkrs(ws + OFF_AT + (size_t)dn * 72 * 4096, 72 * 4096);
#pragma unroll
      for (int p = 0; p < 9; ++p) dma16c(rA, smem + L_AT + p * 1024, (lane >> 3) * 4096 + (lane & 7) * 16, p * 8 * 4096 + t3 * 128);
      const rsrc_t rG = mkrs(ws + OFF_GATE + t3 * 256, 256);
      dma16c(rG, smem + L_AT + 9216, v16, 0);
    } else if (ssm) {
      const rsrc_t rA = mkrs(ws + OFF_AT + (size_t)(36 * tn + 4 * ts) * 4096, 4 * 4096);
#pragma unroll 4
      for (int p = 0; p < 16; ++p) st128(*(LAS u32x4*)(smem + L_US + (p >> 2) * UCP + (p & 3) * 1024 + v16), rA, v16 + (p * 1024));
      WAITV(0);
      if (lane == 0) __hip_atomic_store((GAS unsigned*)ctl + C_FA + (tn >> 1) * 64 + (tn & 1) * 9 + ts, E, RLX);
    }
    if (lw == 1) BAR();
    WAITV(0);
    BAR();
    BAR();
    return;
  }

  relane();
  const int dch = 36 * tn + 4 * ts + wid;
  const int dsf = ssm ? dch : 0;
  const float dskf = __uint_as_float((((const unsigned*)dsk)[dsf >> 1] >> (16 * (dsf & 1))) << 16);
  const int rh = wid >> 1, ch = wid & 1;
  int xo[2][2], wo[5];
#pragma unroll
  for (int kk = 0; kk < 2; ++kk)
#pragma unroll
    for (int h = 0; h < 2; ++h) xo[kk][h] = (64 * rh + 32 * (h ^ ch) + fr) * 128 + (((4 * kk + fq) ^ ((fr >> 1) & 7)) << 4);
  { const int q4 = fr >> 2, p4 = lane & 3, cb = ((288 * tn) & 127) >> 4, sz = (q4 & 2) | ((fq & 1) << 2);
#pragma unroll
    for (int b = 0; b < 5; ++b) wo[b] = 16384 + (8 * fq + q4) * 384 + (((cb + 2 * (b & 3) + (p4 >> 1)) ^ sz) << 4) + 8 * (p4 & 1) + (b < 4 ? 128 * ch : 256); }
  asm volatile("" : "+v"(xo[0][0]), "+v"(xo[0][1]), "+v"(xo[1][0]), "+v"(xo[1][1]), "+v"(wo[0]), "+v"(wo[1]), "+v"(wo[2]), "+v"(wo[3]), "+v"(wo[4]));
  auto rdz = [&](ZF& F, int sl, int kk) {
    LAS char* s = smem + sl;
#pragma unroll
    for (int i = 0; i < 4; ++i) F.x[i] = *(LAS bf16x8*)(s + xo[kk][i >> 1] + (i & 1) * 2048);
#pragma unroll
    for (int b = 0; b < 5; ++b)
#pragma unroll
      for (int t = 0; t < 2; ++t) F.w[b][t] = __builtin_amdgcn_ds_read_tr16_b64_v4i16((LAS s16x4*)(s + wo[b] + (32 * kk + 4 * t) * 384));
  };
  f32x4 za[4][4], z8[2];
  auto zzero = [&]() {
#pragma unroll
    for (int i = 0; i < 4; ++i)
#pragma unroll
      for (int b = 0; b < 4; ++b) za[i][b] = (f32x4){0, 0, 0, 0};
    z8[0] = (f32x4){0, 0, 0, 0}; z8[1] = (f32x4){0, 0, 0, 0}; };
  auto mmz = [&](const ZF& F, bool z2, bool xw = false) {
#pragma unroll
    for (int b = 0; b < 5; ++b) { const bf16x8 wv = __builtin_shufflevector(F.w[b][0], F.w[b][1], 0, 1, 2, 3, 4, 5, 6, 7);
#pragma unroll
      for (int i = 0; i < (b < 4 ? 4 : 2); ++i) { f32x4& a = b < 4 ? za[i][b & 3] : z8[i & 1];
        a = z2 ? __builtin_amdgcn_mfma_f32_16x16x32_bf16(F.x[i], wv, a, 0, 0, 0) : __builtin_amdgcn_mfma_f32_16x16x32_bf16(wv, F.x[i], a, 0, 0, 0); } }
    if (!xw) {
#pragma unroll
      for (int i = 0; i < 14; ++i) { __builtin_amdgcn_sched_group_barrier(0x8, 1, 0); __builtin_amdgcn_sched_group_barrier(0x100, 1, 0); }
      __builtin_amdgcn_sched_group_barrier(0x8, 4, 0);
    } else {
#pragma unroll
      for (int i = 0; i < 18; ++i) { __builtin_amdgcn_sched_group_barrier(0x8, 1, 0); __builtin_amdgcn_sched_group_barrier(0x100, 1, 0); __builtin_amdgcn_sched_group_barrier(0x2, 3, 0); }
    }
    SB0();
  };
  ZF F0, F1;
  int sl = 0;
  auto zh1 = [&](bool z2) { rdz(F1, sl, 1); mmz(F0, z2); };
  auto zbar = [&]() { WAITL0(); BAR(); sl = (sl == 2 * ZST) ? 0 : sl + ZST; asm volatile("" : "+s"(sl)); };
  auto zh2 = [&](bool z2) { rdz(F0, sl, 0); mmz(F1, z2); };
  zzero();
  BAR();
  rdz(F0, 0, 0);
#pragma unroll 1
  for (int g = 0; g < 9; ++g) {
    zh1(true);
    zbar(); zh2(true);
  }
  relane();
  u32x4 zr[2][4], zr8;
#pragma unroll
  for (int P = 0; P < 2; ++P) {
    f32x2 S[4], Q[4];
#pragma unroll
    for (int k = 0; k < 4; ++k) { S[k] = (f32x2){0, 0}; Q[k] = (f32x2){0, 0}; }
#pragma unroll
    for (int b = 0; b < (P ? 4 : 5); ++b)
#pragma unroll
      for (int k = 0; k < 4; ++k) { const f32x4 c = b < 4 ? za[2 * P + (k >> 1)][b & 3] : z8[k >> 1]; f32x2 v = (k & 1) ? (f32x2){c[2], c[3]} : (f32x2){c[0], c[1]};
        v = gelu2(v); S[k] += v; Q[k] += v * v; if (b < 4) zr[P][b & 3][k] = pk2(v.x, v.y); else zr8[k] = pk2(v.x, v.y); }
#pragma unroll
    for (int k = 0; k < 4; ++k) { S[k].x = rowsum16(S[k].x); S[k].y = rowsum16(S[k].y); Q[k].x = rowsum16(Q[k].x); Q[k].y = rowsum16(Q[k].y); }
    if (fr == 0) {
      LAS f32x4* d = (LAS f32x4*)(smem + L_PS + ch * 1024 + (64 * rh + 32 * (P ^ ch) + 8 * fq) * 4);
      d[0] = (f32x4){S[0].x, S[0].y, S[1].x, S[1].y}; d[1] = (f32x4){S[2].x, S[2].y, S[3].x, S[3].y};
      d[32] = (f32x4){Q[0].x, Q[0].y, Q[1].x, Q[1].y}; d[33] = (f32x4){Q[2].x, Q[2].y, Q[3].x, Q[3].y};
    }
  }
#pragma unroll
  for (int b = 0; b < 4; ++b) asm volatile("" : "+v"(zr[0][b]), "+v"(zr[1][b]));
  asm volatile("" : "+v"(zr8));
  zzero();
  zh1(false); zbar();
  const unsigned E = __builtin_amdgcn_readfirstlane(*(volatile LAS unsigned*)(smem + L_EPO));
  {
    const int row = 32 * wid + 8 * fq + (fr & 7);
    LAS float* ps = (LAS float*)(smem + L_PS) + (fr >> 3) * 128 + row;
    GAS unsigned long long* gp = (GAS unsigned long long*)(ws + OFF_ST + ((tm * 16 + tn) * 128 + row) * 16 + (fr >> 3) * 8);
    __hip_atomic_store(gp, ((unsigned long long)E << 32) | __float_as_uint(ps[0] + ps[256]), RLX);
  }
  zh2(false);
#pragma unroll 1
  for (int g = 1; g < RNG - 9; ++g) { zh1(false); zbar(); zh2(false); }
  {
    zh1(false);
    zbar();
    const rsrc_t rZs = mkrs(ws + OFF_Z2, SZ_F);
    f32x4 rn[2][4];
#pragma unroll
    for (int P = 0; P < 2; ++P)
#pragma unroll
      for (int k = 0; k < 4; ++k) rn[P][k] = *(LAS f32x4*)(smem + L_RN + (k >> 1) * 512 + (64 * rh + 32 * (P ^ ch) + 8 * fq) * 4 + (k & 1) * 16);
    const int zo = ((2 * tm + rh) * 144 + 9 * tn) * 2048 + v16;
    auto nrm = [&](int n0) {
#pragma unroll
      for (int n = n0; n < n0 + 3; ++n) { const int P = n < 5 ? 0 : 1, b = n < 5 ? n : n - 5; const u32x4 z = b < 4 ? zr[P][b & 3] : zr8; u32x4 o;
#pragma unroll
        for (int k = 0; k < 4; ++k) o[k] = pk2(bflo(z[k]) * rn[P][k >> 1][2 * (k & 1)] + rn[P][2 + (k >> 1)][2 * (k & 1)], bfhi(z[k]) * rn[P][k >> 1][2 * (k & 1) + 1] + rn[P][2 + (k >> 1)][2 * (k & 1) + 1]);
        __builtin_amdgcn_raw_buffer_store_b128(o, rZs, zo + (P ^ ch) * 1024 + (b < 4 ? 4 * ch + b : 8) * 2048, 0, 16); }
    };
    SB0();
    rdz(F0, sl, 0); nrm(0); mmz(F1, false, true);
    rdz(F1, sl, 1); nrm(3); mmz(F0, false, true);
    zbar();
    rdz(F0, sl, 0); nrm(6); mmz(F1, false, true);
  }
#pragma unroll 1
  for (int g = RNG - 7; g < 9; ++g) {
    zh1(false);
    if (g == FS) { WAITV(0); if (lane == 0) __hip_atomic_store((GAS unsigned*)ctl + C_F2 + tn * 64 + tm * 4 + wid, E, RLX); }
    zbar(); zh2(false);
  }
  relane();
  u32x2 y1[4][4], y8[2];
#pragma unroll
  for (int i = 0; i < 4; ++i)
#pragma unroll
    for (int b = 0; b < (i < 2 ? 5 : 4); ++b) { const f32x4 cc = b < 4 ? za[i][b & 3] : z8[i & 1];
      const f32x2 v0 = gelu2((f32x2){cc[0], cc[1]}), v1 = gelu2((f32x2){cc[2], cc[3]}); u32x2 o = {pk2(v0.x, v0.y), pk2(v1.x, v1.y)}; asm volatile("" : "+v"(o));
      if (b < 4) y1[i][b & 3] = o; else y8[i & 1] = o; }
#pragma unroll
  for (int i = 0; i < 4; ++i) { const float bv = bf2f(*((LAS u16*)(smem + L_BIAS) + 64 * rh + 32 * ((i >> 1) ^ ch) + 16 * (i & 1) + fr));
#pragma unroll
    for (int b = 0; b < 4; ++b) za[i][b] = (f32x4){bv, bv, bv, bv};
    if (i < 2) z8[i] = (f32x4){bv, bv, bv, bv}; }
  {
    struct GF { bf16x8 w[4], z[5]; } G0, G1;
    const int zoA = 16384 + ch * 8192 + v16, zo8 = 32768 + v16;
    auto rd = [&](GF& G, int kk) {
      LAS char* s = smem + sl;
#pragma unroll
      for (int i = 0; i < 4; ++i) G.w[i] = *(LAS bf16x8*)(s + xo[kk][i >> 1] + (i & 1) * 2048);
#pragma unroll
      for (int b = 0; b < 5; ++b) G.z[b] = *(LAS bf16x8*)(s + (b < 4 ? zoA + b * 2048 : zo8) + kk * 1024);
    };
    auto mm = [&](const GF& G) {
#pragma unroll
      for (int b = 0; b < 5; ++b)
#pragma unroll
        for (int i = 0; i < (b < 4 ? 4 : 2); ++i) { f32x4& a = b < 4 ? za[i][b & 3] : z8[i & 1]; a = __builtin_amdgcn_mfma_f32_16x16x32_bf16(G.z[b], G.w[i], a, 0, 0, 0); }
#pragma unroll
      for (int i = 0; i < GP1; ++i) { __builtin_amdgcn_sched_group_barrier(0x8, 1, 0); __builtin_amdgcn_sched_group_barrier(0x100, 1, 0); }
#pragma unroll
      for (int i = GP1; i < 9; ++i) { __builtin_amdgcn_sched_group_barrier(0x8, 2, 0); __builtin_amdgcn_sched_group_barrier(0x100, 1, 0); }
      __builtin_amdgcn_sched_group_barrier(0x8, 18, 0); SB0();
    };
    WAITL0(); BAR();
    sl = 0;
    rd(G0, 0);
#pragma unroll 1
    for (int i = 0; i < 32; ++i) {
      rd(G1, 1); mm(G0);
      WAITL0(); BAR();
      sl = (sl == 3 * GST) ? 0 : sl + GST;
      rd(G0, 0); mm(G1);
    }
    relane();
  }
  {
    const rsrc_t rHs = mkrs(ws + OFF_H + (size_t)((tm * 8 + 4 * rh) * 72 + 9 * pp) * 1024, 4 * 72 * 1024);
    auto hm = [](const u32x2 y, const f32x4 g) { return (u32x2){pk2(bflo(y[0]) * g[0], bfhi(y[0]) * g[1]), pk2(bflo(y[1]) * g[2], bfhi(y[1]) * g[3])}; };
#pragma unroll
    for (int i = 0; i < 4; ++i) { const int to = ((i + 2 * ch) & 3) * 72 * 1024;
#pragma unroll
      for (int c = 0; c < 2; ++c) { const u32x2 lo = hm(y1[i][2 * c], za[i][2 * c]), hi = hm(y1[i][2 * c + 1], za[i][2 * c + 1]);
        st128((u32x4){lo[0], lo[1], hi[0], hi[1]}, rHs, v16 + to + (4 * ee + 2 * ch + c) * 1024); }
      if (i < 2) __builtin_amdgcn_raw_buffer_store_b64(hm(y8[i], z8[i]), rHs, to + 8192 + ee * 512 + (fr + 16 * (fq >> 1)) * 16 + (fq & 1) * 8, 0, 16);
    }
  }
  WAITL0(); BAR();
  if (ssm) {
    LAS char* ub = smem + L_US + wid * UCP;
    const int sg = (fr + 1) & 3;
    LAS char* pA = smem + l_kcp(wid) + (sg == 0 ? cb0 : sg == 1 ? cb1 : sg == 2 ? cb2 : cb3) + 2 * (8 * (fr >> 2) + 8 * fq + (((fr & 3) + 1) & 4));
    LAS char* pB = ub + 96 * (14 + fr) + 16 * fq;
    f32x4 ya[4][2];
#pragma unroll
    for (int c = 0; c < 4; ++c) { ya[c][0] = (f32x4){0, 0, 0, 0}; ya[c][1] = (f32x4){0, 0, 0, 0}; }
    {
      u32x4 ra[2][4], rb[2][4]; u32x2 rh2[2][4];
      auto rd1 = [&](int s, int kk, int n) { const int e = n / 3;
        if (n % 3 == 0) rb[s][e] = *(volatile LAS u32x4*)(pB + 96 * (1 - kk) + 1536 * e);
        if (n % 3 == 1) ra[s][e] = *(volatile LAS u32x4*)(pA + 64 * kk + 1024 * e);
        if (n % 3 == 2) rh2[s][e] = *(volatile LAS u32x2*)(pA + 64 * kk + 1024 * e + 16); };
      auto step = [&](int s, int kk) {
        u32x4 a1[4];
#pragma unroll
        for (int e = 0; e < 4; ++e) { a1[e] = (u32x4){ra[s][e][2], ra[s][e][3], rh2[s][e][0], rh2[s][e][1]};
          asm volatile("" : "+v"(a1[e]), "+v"(ra[s][e]), "+v"(rb[s][e])); }
        int n = 0;
#pragma unroll
        for (int e = 0; e < 4; ++e)
#pragma unroll
          for (int t = 0; t < 2; ++t)
#pragma unroll
            for (int c = e; c < 4; ++c) {
              asm volatile("v_mfma_f32_16x16x32_bf16 %0, %1, %2, %0" : "+v"(ya[c][t]) : "v"(t ? a1[e] : ra[s][e]), "v"(rb[s][c - e]));
              if (n < 12) rd1(s ^ 1, kk, n);
              ++n; }
      };
#pragma unroll
      for (int n = 0; n < 12; ++n) rd1(0, 0, n);
      asm volatile("s_nop 1" : "+v"(ya[0][0]), "+v"(ya[0][1]), "+v"(ya[1][0]), "+v"(ya[1][1]), "+v"(ya[2][0]), "+v"(ya[2][1]), "+v"(ya[3][0]), "+v"(ya[3][1]));
#pragma unroll 1
      for (int g = 0; g < 8; ++g) {
        step(0, 1);
        pA += 128; pB -= 192;
        step(1, 0);
        if (g == HFG) {
          WAITV(0);
          if (lane == 0) __hip_atomic_store((GAS unsigned*)ctl + C_F3 + tm * 64 + tn * 4 + wid, E, RLX);
        }
      }
      asm volatile("s_nop 7\n\ts_nop 3" : "+v"(ya[0][0]), "+v"(ya[0][1]), "+v"(ya[1][0]), "+v"(ya[1][1]), "+v"(ya[2][0]), "+v"(ya[2][1]), "+v"(ya[3][0]), "+v"(ya[3][1]));
    }
    u32x4 uu[4];
#pragma unroll
    for (int c = 0; c < 4; ++c) { uu[c] = *(LAS u32x4*)(ub + 96 * (15 + 16 * c + fr) + 16 * (3 - fq)); asm volatile("" : "+v"(uu[c])); }
#pragma unroll
    for (int c = 0; c < 4; ++c) { u32x4 o;
      o[0] = pk2(ya[c][0][0] + bfhi(uu[c][3]) * dskf, ya[c][0][1] + bflo(uu[c][3]) * dskf); o[1] = pk2(ya[c][0][2] + bfhi(uu[c][2]) * dskf, ya[c][0][3] + bflo(uu[c][2]) * dskf);
      o[2] = pk2(ya[c][1][0] + bfhi(uu[c][1]) * dskf, ya[c][1][1] + bflo(uu[c][1]) * dskf); o[3] = pk2(ya[c][1][2] + bfhi(uu[c][0]) * dskf, ya[c][1][3] + bflo(uu[c][0]) * dskf);
      *(LAS u32x4*)(ub + c * 1024 + fr * 64 + fq * 16) = o; }
  } else {
    WAITV(0);
    if (lane == 0) __hip_atomic_store((GAS unsigned*)ctl + C_F3 + tm * 64 + tn * 4 + wid, E, RLX);
  }
  relane();
  constexpr int DEPTH = 3;
  {
    const rsrc_t rH = mkrs(ws + OFF_H + (size_t)t3 * 4 * 72 * 1024, 4 * 72 * 1024);
    const rsrc_t rF = mkrs(ws + OFF_WF + (size_t)dn * 72 * 4608, 72 * 4608);
    const int vh = 4096 + (fq * 8 + (fr & 7)) * 16;
    bf16x8 H[DEPTH][4], W[DEPTH][5];
    auto ldH = [&](int s, int j) { const int P = j * 4 + wid;
#pragma unroll
      for (int b = 0; b < 4; ++b) H[s][b] = __builtin_bit_cast(bf16x8, __builtin_amdgcn_raw_buffer_load_b128(rH, v16, P * 1024 + ((b + wid) & 3) * 72 * 1024, AUXH)); };
    auto ldW = [&](int s, int j) { const int P = j * 4 + wid;
      if (j < JL || j >= 18 - JR) { LAS char* p = smem + (j < JL ? l_wpf(j) : GST + (j - (18 - JR)) * 18432) + wid * 4608;
#pragma unroll
        for (int c = 0; c < 4; ++c) W[s][c] = *(LAS bf16x8*)(p + v16 + c * 1024);
        W[s][4] = *(LAS bf16x8*)(p + vh);
      } else {
#pragma unroll
        for (int c = 0; c < 4; ++c) W[s][c] = __builtin_bit_cast(bf16x8, __builtin_amdgcn_raw_buffer_load_b128(rF, v16 + c * 1024, P * 4608, AUXW));
        W[s][4] = __builtin_bit_cast(bf16x8, __builtin_amdgcn_raw_buffer_load_b128(rF, vh, P * 4608, AUXW));
      } };
    WAITL0(); BAR();
#pragma unroll
    for (int s = 0; s < DEPTH; ++s) { ldH(s, s); ldW(s, s); }
    SB0();
    f32x4 ac3[4][5];
#pragma unroll
    for (int b = 0; b < 4; ++b)
#pragma unroll
      for (int c = 0; c < 5; ++c) ac3[b][c] = (f32x4){0, 0, 0, 0};
#pragma unroll
    for (int j = 0; j < 18; ++j) {
      const int s = j % DEPTH;
#pragma unroll
      for (int b = 0; b < 4; ++b)
#pragma unroll
        for (int c = 0; c < 5; ++c) ac3[b][c] = __builtin_amdgcn_mfma_f32_16x16x32_bf16(H[s][b], W[s][c], ac3[b][c], 0, 0, 0);
      if (j == 18 - JR - DEPTH) BAR();
      if (j + DEPTH < 18) { ldH(s, j + DEPTH); ldW(s, j + DEPTH); }
      SB0();
    }
    WAITL0(); BAR();
#pragma unroll
    for (int b = 1; b < 4; ++b) {
      LAS char* dst = smem + (((b + wid) & 3) * 3 + b - 1) * 5120 + v16;
#pragma unroll
      for (int c = 0; c < 5; ++c) *(LAS f32x4*)(dst + c * 1024) = ac3[b][c];
    }
    WAITL0(); BAR();
    LAS char* src = smem + wid * 3 * 5120 + v16;
    const f32x4 gv = *(LAS f32x4*)(smem + L_AT + 9216 + (wid * 16 + fq * 4) * 4);
    LAS char* ot = smem + 65536 + wid * 2560;
#pragma unroll
    for (int c = 0; c < 5; ++c) {
      const f32x4 v = ac3[0][c] + *(LAS f32x4*)(src + c * 1024) + (*(LAS f32x4*)(src + 5120 + c * 1024) + *(LAS f32x4*)(src + 10240 + c * 1024));
      const u32x2 av = *(LAS u32x2*)(smem + L_AT + (c * 16 + (c < 4 ? fr : (fr & 7))) * 128 + (wid * 16 + fq * 4) * 2);
      const unsigned p0 = pk2(gv[0] * bflo(av[0]) + (1.f - gv[0]) * v[0], gv[1] * bfhi(av[0]) + (1.f - gv[1]) * v[1]);
      const unsigned p1 = pk2(gv[2] * bflo(av[1]) + (1.f - gv[2]) * v[2], gv[3] * bfhi(av[1]) + (1.f - gv[3]) * v[3]);
      LAS u16* o = (LAS u16*)(ot + fq * 640 + (c * 16 + fr) * 2);
      o[0] = (u16)p0; o[80] = (u16)(p0 >> 16); o[160] = (u16)p1; o[240] = (u16)(p1 >> 16);
    }
#pragma unroll
    for (int k = 0; k < 3; ++k) { const int id = k * 64 + lane, row = id / 9, cc = id - row * 9;
      if (id < 144) __builtin_amdgcn_raw_buffer_store_b128(*(LAS u32x4*)(ot + row * 160 + cc * 16), mkrs(out + (size_t)(t3 * 64 + wid * 16) * 576, 16 * 1152), row * 1152 + dn * 144 + cc * 16, 0, OAUX); }
  }
}

extern "C" void kernel_launch(void* const* d_in, const int* in_sizes, int n_in, void* d_out, int out_size, void* d_ws, size_t ws_size, hipStream_t stream) {
  (void)in_sizes; (void)n_in; (void)out_size; (void)ws_size;
  (void)hipFuncSetAttribute((const void*)gmix_fused, hipFuncAttributeMaxDynamicSharedMemorySize, LDS_BYTES);
  gmix_fused<<<256, 512, LDS_BYTES, stream>>>((const u16*)d_in[0], (const u16*)d_in[1], (const u16*)d_in[2], (const u16*)d_in[3], (const u16*)d_in[4], (const u16*)d_in[5],
      (const u16*)d_in[6], (const u16*)d_in[8], (const u16*)d_in[9], (const u16*)d_in[10], (const u16*)d_in[11], (const u16*)d_in[12], (u16*)d_out, (char*)d_ws);
}
```
